```python
import torch

def _softmax(x, axis=-1):
    return torch.softmax(x, dim=axis)

def _gelu(x):
    return torch.nn.functional.gelu(x, approximate='tanh')

def _silu(x):
    return torch.nn.functional.silu(x)

def _sigmoid(x):
    return torch.sigmoid(x)

def _layernorm(x, axis=-1, eps=1e-05):
    m = x.mean(dim=axis, keepdim=True)
    v = x.var(dim=axis, keepdim=True, unbiased=False)
    return (x - m) / torch.sqrt(v + eps)

def _scatter_add(tgt, idx, src, axis=0):
    return tgt.clone().index_add_(axis, idx, src)

def _segment_sum(src, seg, n):
    out = torch.zeros((n,) + src.shape[1:], dtype=src.dtype)
    return out.index_add_(0, seg, src)

def _pad(x, pw):
    flat = tuple((v for pair in reversed(pw) for v in pair))
    return torch.nn.functional.pad(x, flat)
kernel_name = 'adaLN_cond__linear_kernel_hedgehog__dense_activation_relu__prompt'

def setup_inputs():

    _w = lambda a: (lambda t: t.to(torch.bfloat16) if t.is_floating_point() else t)(torch.as_tensor(a))

    def _A_setup():
        B, S, D, H = (1, 2048, 512, 32)
        Dh = D // H
        return {'x': _w((lambda *s: torch.randn(*s, dtype=torch.bfloat16) * 0.05)(B, S, D)), 'wqkv': _w((lambda *s: torch.randn(*s, dtype=torch.bfloat16) * 0.4)(D, 3 * D)), 'w_phi': _w((lambda *s: torch.randn(*s, dtype=torch.bfloat16) * 0.4)(H, Dh, Dh) * Dh ** (-0.5)), 'wo': _w((lambda *s: torch.randn(*s, dtype=torch.bfloat16) * 0.05)(D, D))}
    _A_in = _A_setup()

    def _B_setup():
        B, S, D, Dff = (1, 2048, 512, 1376)
        return {'x': _w((lambda *s: torch.randn(*s, dtype=torch.bfloat16) * 0.05)(B, S, D)), 'w1': _w((lambda *s: torch.randn(*s, dtype=torch.bfloat16) * 0.05)(D, Dff)), 'w2': _w((lambda *s: torch.randn(*s, dtype=torch.bfloat16) * 0.05)(Dff, D))}
    _B_in = _B_setup()
    return {'A_x': _A_in['x'], 'A_wqkv': _A_in['wqkv'], 'A_w_phi': _A_in['w_phi'], 'A_wo': _A_in['wo'], 'B_x': _B_in['x'], 'B_w1': _B_in['w1'], 'B_w2': _B_in['w2'], 'c': _w((lambda *s: torch.randn(*s, dtype=torch.bfloat16) * 0.1)(1, 512) * 8.0), 'w_mod': _w((lambda *s: torch.randn(*s, dtype=torch.bfloat16) * 0.1)(512, 6 * 512) * 512 ** (-0.5) * 8.0)}

def reference(A_x, A_wqkv, A_w_phi, A_wo, B_x, B_w1, B_w2, c, w_mod):

    def _A_ref(x, wqkv, w_phi, wo):
        B, S, D = x.shape
        H = 32
        Dh = D // H
        qkv = (lambda x, *d: x.permute(*d))((x @ wqkv).reshape(B, S, 3, H, Dh), 2, 0, 3, 1, 4)
        q, k, v = (qkv[0], qkv[1], qkv[2])
        pq = torch.einsum('bhsd,hdf->bhsf', q, w_phi)
        pk = torch.einsum('bhsd,hdf->bhsf', k, w_phi)
        phi_q = torch.concatenate([_softmax(pq, axis=-1), _softmax(-pq, axis=-1)], axis=-1)
        phi_k = torch.concatenate([_softmax(pk, axis=-1), _softmax(-pk, axis=-1)], axis=-1)
        kv = torch.einsum('bhsf,bhsd->bhfd', phi_k, v)
        z = torch.einsum('bhsf->bhf', phi_k)
        num = torch.einsum('bhqf,bhfd->bhqd', phi_q, kv)
        den = torch.einsum('bhqf,bhf->bhq', phi_q, z)[..., None] + 1e-06
        o = num / den
        return (lambda x, *d: x.permute(*d))(o, 0, 2, 1, 3).reshape(B, S, D) @ wo

    def _B_ref(x, w1, w2):
        h = torch.einsum('bsd,df->bsf', x, w1)
        zero = torch.zeros_like(h)
        h = torch.maximum(h, zero)
        return torch.einsum('bsf,fd->bsd', h, w2)
    x = A_x
    mod = (_silu(c) @ w_mod).reshape(c.shape[0], 6, 1, -1)
    sa, ca, ga, sb, cb, gb = (mod[:, 0].clone(), mod[:, 1].clone(), mod[:, 2].clone(), mod[:, 3].clone(), mod[:, 4].clone(), mod[:, 5].clone())
    h = _layernorm(x, axis=-1) * (1 + ca) + sa
    x = x + ga * _A_ref(h, A_wqkv, A_w_phi, A_wo)
    h = _layernorm(x, axis=-1) * (1 + cb) + sb
    x = x + gb * _B_ref(h, B_w1, B_w2)
    return x
```

```cpp
#include <hip/hip_runtime.h>
#include <hip/hip_bf16.h>
typedef __hip_bfloat16 bf16;
static __device__ __forceinline__ float b2f(bf16 x){return __bfloat162float(x);}
static __device__ __forceinline__ float rb(float x){return __bfloat162float(__float2bfloat16(x));}
#define S 2048
#define D 512
#define H 32
#define DFF 1376

__global__ void k_mod(const bf16* c, const bf16* wmod, float* mod){
  int n = blockIdx.x*256+threadIdx.x;
  float acc=0;
  for(int k=0;k<D;k++){ float v=b2f(c[k]); float s=rb(v/(1.f+__expf(-v))); acc+=s*b2f(wmod[k*3072+n]); }
  mod[n]=rb(acc);
}
__global__ void k_ln(const float* xin, const bf16* xin16, const float* shift, const float* scale, float* h){
  int r=blockIdx.x; int t=threadIdx.x; __shared__ float sm[512]; __shared__ float red[2];
  float v = xin? xin[r*D+t] : b2f(xin16[r*D+t]); sm[t]=v; __syncthreads();
  if(t==0){float m=0; for(int i=0;i<D;i++) m+=sm[i]; m/=D; float q=0; for(int i=0;i<D;i++) q+=(sm[i]-m)*(sm[i]-m); q/=D; red[0]=m; red[1]=rsqrtf(q+1e-5f);}
  __syncthreads();
  h[r*D+t]=rb((v-red[0])*red[1]*(1.f+scale[t])+shift[t]);
}
__global__ void k_gemm(const float* A, const bf16* B, float* C, int K, int N, int relu){
  int r=blockIdx.y; int n=blockIdx.x*256+threadIdx.x; if(n>=N) return;
  float acc=0; for(int k=0;k<K;k++) acc+=A[r*K+k]*b2f(B[k*N+n]);
  acc=rb(acc); if(relu) acc=fmaxf(acc,0.f);
  C[r*N+n]=acc;
}
__global__ void k_phi(const float* qkv, const bf16* wphi, float* phiq, float* phik){
  int idx=blockIdx.x*256+threadIdx.x;
  int s=idx/64, w=(idx/32)&1, h=idx&31;
  const float* q=qkv+s*1536+w*512+h*16;
  float p[16]; float mx=-1e30f, mn=1e30f;
  #pragma unroll
  for(int f=0;f<16;f++){ float a=0;
    #pragma unroll
    for(int d=0;d<16;d++) a+=q[d]*b2f(wphi[h*256+d*16+f]); p[f]=rb(a); mx=fmaxf(mx,p[f]); mn=fminf(mn,p[f]);}
  float s1=0,s2=0;
  #pragma unroll
  for(int f=0;f<16;f++){ s1+=__expf(p[f]-mx); s2+=__expf(mn-p[f]); }
  float* o=(w?phik:phiq)+(s*32+h)*32;
  #pragma unroll
  for(int f=0;f<16;f++){ o[f]=rb(__expf(p[f]-mx)/s1); o[16+f]=rb(__expf(mn-p[f])/s2);}
}
__global__ void k_kv(const float* phik, const float* qkv, float* kv, float* z){
  int h=blockIdx.x; int t=threadIdx.x; int f=t/16, d=t&15;
  float a=0, zz=0;
  for(int s=0;s<S;s++){ float p=phik[(s*32+h)*32+f]; a+=p*qkv[s*1536+1024+h*16+d]; zz+=p; }
  kv[h*512+t]=rb(a); if(d==0) z[h*32+f]=rb(zz);
}
__global__ void k_o(const float* phiq, const float* kv, const float* z, float* o){
  int s=blockIdx.x; int t=threadIdx.x; int h=t/16,d=t&15;
  float num=0,den=0;
  for(int f=0;f<32;f++){ float p=phiq[(s*32+h)*32+f]; num+=p*kv[h*512+f*16+d]; den+=p*z[h*32+f]; }
  o[s*D+t]=rb(rb(num)/rb(rb(den)+1e-6f));
}
__global__ void k_res(const bf16* x16, const float* xf, const float* g, const float* y, float* outf, bf16* out16){
  int r=blockIdx.x,t=threadIdx.x;
  float x = x16? b2f(x16[r*D+t]) : xf[r*D+t];
  float v = rb(x+rb(g[t]*y[r*D+t]));
  if(outf) outf[r*D+t]=v; else out16[r*D+t]=__float2bfloat16(v);
}
extern "C" void kernel_launch(void* const* d_in, const int* in_sizes, int n_in,
                              void* d_out, int out_size, void* d_ws, size_t ws_size,
                              hipStream_t stream) {
  const bf16* x=(const bf16*)d_in[0]; const bf16* wqkv=(const bf16*)d_in[1]; const bf16* wphi=(const bf16*)d_in[2];
  const bf16* wo=(const bf16*)d_in[3]; const bf16* w1=(const bf16*)d_in[5]; const bf16* w2=(const bf16*)d_in[6];
  const bf16* c=(const bf16*)d_in[7]; const bf16* wmod=(const bf16*)d_in[8];
  float* ws=(float*)d_ws;
  float* mod=ws; float* h=ws+4096; float* qkv=h+S*D; float* phiq=qkv+S*1536; float* phik=phiq+S*1024; float* kv=phik+S*1024; float* z=kv+32*512;
  float* o=z+1024; float* y=o+S*D; float* x1=y+S*D; float* f=x1+S*D;
  k_mod<<<12,256,0,stream>>>(c,wmod,mod);
  k_ln<<<S,512,0,stream>>>(nullptr,x,mod,mod+512,h);
  k_gemm<<<dim3(6,S),256,0,stream>>>(h,wqkv,qkv,512,1536,0);
  k_phi<<<S*64/256,256,0,stream>>>(qkv,wphi,phiq,phik);
  k_kv<<<32,512,0,stream>>>(phik,qkv,kv,z);
  k_o<<<S,512,0,stream>>>(phiq,kv,z,o);
  k_gemm<<<dim3(2,S),256,0,stream>>>(o,wo,y,512,512,0);
  k_res<<<S,512,0,stream>>>(x,nullptr,mod+1024,y,x1,nullptr);
  k_ln<<<S,512,0,stream>>>(x1,nullptr,mod+1536,mod+2048,h);
  k_gemm<<<dim3(6,S),256,0,stream>>>(h,w1,f,512,DFF,1);
  k_gemm<<<dim3(2,S),256,0,stream>>>(f,w2,y,DFF,512,0);
  k_res<<<S,512,0,stream>>>(nullptr,x1,mod+2560,y,nullptr,(bf16*)d_out);
}
```

```cpp
#include <hip/hip_runtime.h>

typedef unsigned u32;
typedef unsigned long long u64;
typedef u32 v4u __attribute__((ext_vector_type(4)));
typedef u32 v2u __attribute__((ext_vector_type(2)));
typedef float v16f __attribute__((ext_vector_type(16)));
typedef float v4f __attribute__((ext_vector_type(4)));
typedef float v2f __attribute__((ext_vector_type(2)));
typedef __bf16 v8bf __attribute__((ext_vector_type(8)));
typedef __bf16 v4bf __attribute__((ext_vector_type(4)));
typedef __bf16 v2bf __attribute__((ext_vector_type(2)));
typedef short v2s __attribute__((ext_vector_type(2)));
typedef __amdgpu_buffer_rsrc_t rsrc_t;
#define LDS __attribute__((address_space(3)))
#define DEV __device__ __forceinline__
#define UNROLL _Pragma("unroll")

constexpr u32 WS_NONCE = 0;
constexpr u32 WS_FLAG = 0x1000;
constexpr u32 WS_MODP = 0x40000;
constexpr u32 WS_KVP = 0x80000;
constexpr u32 WS_ST = 0x1C0000;
constexpr u32 WS_XN = 0x200000;
constexpr u32 WS_O = 0x400000;
constexpr u32 WS_U = 0x600000;
constexpr u32 WS_F = 0x800000;
constexpr u32 WS_END = 0xE00000;
constexpr u32 WS_STAMP = 0x1000000;
constexpr u32 OOB = 0x80000000u;
constexpr u32 L_WK = 0, L_WV = 32768, L_W1 = 0, L_WQ = 98304, L_RED = 98304, L_SCR = 131072, L_MISC = 147456;
constexpr u32 L_MODRED = L_MISC, L_LNST = L_MISC + 1024, L_BIAS = L_MISC + 1280, L_ZERO = L_MISC + 2816,
              L_WPHI = L_MISC + 3072, L_ZP = L_MISC + 4096, L_KVF = L_MISC + 5120, L_ZF = L_MISC + 7168,
              L_MOD2 = L_MISC + 8192, L_CF = L_MISC + 12288;
constexpr u32 SPIN_CAP = 1u << 18;
constexpr float LOG2E = 1.4426950408889634f;

DEV rsrc_t mk(const void* p, u32 n) { return __builtin_amdgcn_make_buffer_rsrc((void*)p, 0, n, 0x00020000); }
template <int AUX> DEV v4u ld128(rsrc_t r, u32 v, u32 s) { return __builtin_amdgcn_raw_buffer_load_b128(r, v, s, AUX); }
template <int AUX> DEV v2u ld64(rsrc_t r, u32 v, u32 s) { return __builtin_amdgcn_raw_buffer_load_b64(r, v, s, AUX); }
template <int AUX> DEV u32 ld32(rsrc_t r, u32 v, u32 s) { return __builtin_amdgcn_raw_buffer_load_b32(r, v, s, AUX); }
template <int AUX> DEV void st128(v4u d, rsrc_t r, u32 v, u32 s) { __builtin_amdgcn_raw_buffer_store_b128(d, r, v, s, AUX); }
template <int AUX> DEV void st64(v2u d, rsrc_t r, u32 v, u32 s) { __builtin_amdgcn_raw_buffer_store_b64(d, r, v, s, AUX); }
constexpr int SC1 = 16;

DEV float f_lo(u32 x) { return __uint_as_float(x << 16); }
DEV float f_hi(u32 x) { return __uint_as_float(x & 0xffff0000u); }
DEV u32 pk(float a, float b) { v2f t = {a, b}; return __builtin_bit_cast(u32, __builtin_convertvector(t, v2bf)); }
DEV u32 pk2(v2f t) { return __builtin_bit_cast(u32, __builtin_convertvector(t, v2bf)); }
DEV u32 relu2(u32 x) { return __builtin_bit_cast(u32, __builtin_elementwise_max(__builtin_bit_cast(v2s, x), (v2s){0, 0})); }
DEV float rbf(float a) { return f_lo(pk(a, a)); }
DEV float dot2(u32 a, u32 b, float c) { return __builtin_amdgcn_fdot2_f32_bf16(__builtin_bit_cast(v2bf, a), __builtin_bit_cast(v2bf, b), c, false); }
DEV u32 prm_lo(u32 a, u32 b) { return __builtin_amdgcn_perm(b, a, 0x05040100u); }
DEV u32 prm_hi(u32 a, u32 b) { return __builtin_amdgcn_perm(b, a, 0x07060302u); }
DEV v16f mfma(v4u a, v4u b, v16f c) { return __builtin_amdgcn_mfma_f32_32x32x16_bf16(__builtin_bit_cast(v8bf, a), __builtin_bit_cast(v8bf, b), c, 0, 0, 0); }
DEV float swp32_add(float a, float b) { auto r = __builtin_amdgcn_permlane32_swap(__float_as_uint(a), __float_as_uint(b), false, false); return __uint_as_float(r[0]) + __uint_as_float(r[1]); }
DEV float swp16_add(float a, float b) { auto r = __builtin_amdgcn_permlane16_swap(__float_as_uint(a), __float_as_uint(b), false, false); return __uint_as_float(r[0]) + __uint_as_float(r[1]); }
template <int CTRL> DEV float dpp(float a) { return __uint_as_float(__builtin_amdgcn_mov_dpp(__float_as_uint(a), CTRL, 0xf, 0xf, true)); }
DEV void bar() { asm volatile("s_waitcnt lgkmcnt(0)\n\ts_barrier" ::: "memory"); }
DEV void drain() { asm volatile("s_waitcnt vmcnt(0)" ::: "memory"); }
DEV void pin() { asm volatile("" ::: "memory"); __builtin_amdgcn_sched_barrier(0); }

#define SM(T, off) (*(LDS T*)(sm + (off)))
DEV v2u trd(LDS char* p) { return __builtin_bit_cast(v2u, __builtin_amdgcn_ds_read_tr16_b64_v4bf16((LDS v4bf*)p)); }
DEV v4u trd2(LDS char* p0, LDS char* p1) { v2u a = trd(p0), b = trd(p1); v4u r = {a.x, a.y, b.x, b.y}; return r; }

DEV void wait_flags(rsrc_t ws, u32 voff, bool act, v2u tag) {
  for (u32 sp = 0; sp < SPIN_CAP; ++sp) {
    v2u f = ld64<SC1>(ws, act ? voff : OOB, 0);
    if (__all(!act || (f.x == tag.x && f.y == tag.y))) break;
    asm volatile("" ::: "memory");
  }
  pin();
}
DEV void set_flag(rsrc_t ws, u32 phase, u32 id, u32 w, u32 l, v2u tag) {
  drain();
  st64<SC1>(tag, ws, l == 0 ? WS_FLAG + (phase * 256 + id) * 128 + w * 8 : OOB, 0);
  pin();
}
DEV void dma16(v4u srd, u32 voff, u32 soff, u32 ldsdst) {
  asm volatile("s_mov_b32 m0, %0\n\ts_nop 0\n\tbuffer_load_dwordx4 %1, %2, %3 offen lds" ::"s"(ldsdst), "v"(voff), "s"(srd), "s"(soff) : "memory");
}

DEV void features(const v16f& p, u32 (&o)[16]) {
  float mx = p[0], mn = p[0];
  UNROLL for (int i = 1; i < 16; ++i) { mx = fmaxf(mx, p[i]); mn = fminf(mn, p[i]); }
  const float a = -mx * LOG2E, c = mn * LOG2E;
  float e[16], g[16];
  UNROLL for (int i = 0; i < 16; ++i) { e[i] = __builtin_amdgcn_exp2f(fmaf(p[i], LOG2E, a)); g[i] = __builtin_amdgcn_exp2f(fmaf(p[i], -LOG2E, c)); }
  v2f se = {e[0], e[1]}, sg = {g[0], g[1]};
  UNROLL for (int i = 2; i < 16; i += 2) { se += (v2f){e[i], e[i + 1]}; sg += (v2f){g[i], g[i + 1]}; }
  const float re = __builtin_amdgcn_rcpf(se.x + se.y), rg = __builtin_amdgcn_rcpf(sg.x + sg.y);
  UNROLL for (int i = 0; i < 8; ++i) { o[i] = pk2((v2f){e[2 * i], e[2 * i + 1]} * re); o[8 + i] = pk2((v2f){g[2 * i], g[2 * i + 1]} * rg); }
}
DEV v4u pack8(const v16f& a, int s) { v4u r = {pk(a[8 * s], a[8 * s + 1]), pk(a[8 * s + 2], a[8 * s + 3]), pk(a[8 * s + 4], a[8 * s + 5]), pk(a[8 * s + 6], a[8 * s + 7])}; return r; }

DEV float wcol(const v4u (&raw)[8], int t, const v2f (&sc)[4], const v2f (&sa)[4], LDS char* dst) {
  v4u o; v2f bp = {0.f, 0.f};
  UNROLL for (int k = 0; k < 4; ++k) {
    const u32 a = raw[2 * k][t >> 1], b = raw[2 * k + 1][t >> 1];
    const v2f f = (t & 1) ? (v2f){f_hi(a), f_hi(b)} : (v2f){f_lo(a), f_lo(b)};
    o[k] = pk2(f * sc[k]);
    bp += f * sa[k];
  }
  *(LDS v4u*)dst = o;
  return bp.x + bp.y;
}
DEV void bias_red(const float (&bp)[8], u32 l, LDS char* dst) {
  float a[4], e[2];
  UNROLL for (int t = 0; t < 4; ++t) a[t] = swp32_add(bp[t], bp[t + 4]);
  UNROLL for (int t = 0; t < 2; ++t) { e[t] = swp16_add(a[t], a[t + 2]); e[t] += dpp<0x128>(e[t]); e[t] += dpp<0x124>(e[t]); }
  *(LDS float*)dst = (l & 8) ? e[1] : e[0];
}
DEV v4u bias_frag(LDS char* src, u32 h) {
  const v4f q = *(LDS v4f*)src;
  const float s = (q.x + q.y) + (q.z + q.w), hi = rbf(s);
  v4u r = {h ? 0u : pk(hi, s - hi), 0u, 0u, 0u};
  return r;
}

#define STAMP(i)

__global__ void __launch_bounds__(256, 1) fused_block(const void* __restrict__ px, const void* __restrict__ pwqkv, const void* __restrict__ pwphi,
                                                      const void* __restrict__ pwo, const void* __restrict__ pw1, const void* __restrict__ pw2,
                                                      const void* __restrict__ pc, const void* __restrict__ pwmod, void* pout, char* wsp) {
  __shared__ __attribute__((aligned(16))) char smem[163840];
  LDS char* const sm = (LDS char*)smem;
  const u32 b = blockIdx.x, tid = threadIdx.x, l = tid & 63, w = __builtin_amdgcn_readfirstlane(tid >> 6);
  const u32 r = l & 31, h = l >> 5;
  u32 pf;
  {
    u64 pcv; u32 len;
    asm volatile("s_getpc_b64 %0\n.Lpf_here:\n\ts_mov_b32 %1, .Lpf_end-.Lpf_here" : "=s"(pcv), "=s"(len));
    pf = ld32<0>(mk((const void*)pcv, len), tid * 128, 0);
  }
  STAMP(0);
  const rsrc_t rx = mk(px, 2097152), rqkv = mk(pwqkv, 1572864), rphi = mk(pwphi, 16384), rwo = mk(pwo, 524288),
               rw2 = mk(pw2, 1409024), rc = mk(pc, 1024), rmod = mk(pwmod, 3145728), rout = mk(pout, 2097152), ws = mk(wsp, WS_END);
  const u64 nonce = *(const u64*)(wsp + WS_NONCE) + 1;
  const v2u tag = {(u32)nonce, (u32)(nonce >> 32)};
  const u32 xcd = b & 7, j = b >> 3;
  const u32 rg = 4 * (xcd >> 1) + (j >> 3), hp = 8 * (xcd & 1) + (j & 7);
  const u32 rt = 4 * xcd + (j >> 3), ct = j & 7;
  const u32 rg2 = 2 * xcd + (j >> 4), cg = j & 15;
  const u32 tau0 = cg < 11 ? 3 * cg : 11 + 2 * cg;

  const u32 mrow = 128 * (b & 3) + 32 * w + 4 * (l >> 3);
  const v2u cv = ld64<0>(rc, mrow * 2, 0);
  v4u wm[4];
  {
    const u32 vo = b < 192 ? mrow * 6144 + (b >> 2) * 128 + (l & 7) * 16 : OOB;
    UNROLL for (int i = 0; i < 4; ++i) wm[i] = ld128<0>(rmod, vo, i * 6144);
  }
  v4u xr[2];
  UNROLL for (int i = 0; i < 2; ++i) xr[i] = ld128<0>(rx, (8 * b + (l & 7)) * 1024 + (16 * w + (l >> 3)) * 16 + i * 128, 0);
  v4u wk[8], wv[8], wq[8];
  const u32 wvo = (128 * w + 8 * (l >> 2)) * 3072 + hp * 64 + (l & 3) * 16;
  UNROLL for (int i = 0; i < 8; ++i) wk[i] = ld128<0>(rqkv, wvo + 1024, i * 3072);
  UNROLL for (int i = 0; i < 8; ++i) wv[i] = ld128<0>(rqkv, wvo + 2048, i * 3072);
  pin();
  if (tid < 16) SM(u32, L_ZERO + tid * 4) = 0;
  {
    float s[4] = {f_lo(cv.x), f_hi(cv.x), f_lo(cv.y), f_hi(cv.y)};
    UNROLL for (int i = 0; i < 4; ++i) s[i] = s[i] * __builtin_amdgcn_rcpf(1.f + __builtin_amdgcn_exp2f(-LOG2E * s[i]));
    const u32 s01 = pk(s[0], s[1]), s23 = pk(s[2], s[3]);
    float m[8];
    UNROLL for (int t = 0; t < 4; ++t) {
      m[2 * t] = dot2(prm_lo(wm[2][t], wm[3][t]), s23, dot2(prm_lo(wm[0][t], wm[1][t]), s01, 0.f));
      m[2 * t + 1] = dot2(prm_hi(wm[2][t], wm[3][t]), s23, dot2(prm_hi(wm[0][t], wm[1][t]), s01, 0.f));
    }
    float a[4], e[2];
    UNROLL for (int t = 0; t < 4; ++t) a[t] = swp32_add(m[t], m[t + 4]);
    UNROLL for (int t = 0; t < 2; ++t) { e[t] = swp16_add(a[t], a[t + 2]); e[t] += dpp<0x128>(e[t]); }
    const u32 col = 8 * (l & 7) + (l >> 3);
    SM(float, L_MODRED + (col * 4 + w) * 4) = (l & 8) ? e[1] : e[0];
  }
  {
    float s1 = 0.f, s2 = 0.f;
    UNROLL for (int i = 0; i < 2; ++i) UNROLL for (int k = 0; k < 4; ++k) { s1 = dot2(xr[i][k], 0x3f803f80u, s1); s2 = dot2(xr[i][k], xr[i][k], s2); }
    float v = swp32_add(s1, s2);
    v = swp16_add(v, v); v += dpp<0x128>(v);
    SM(float, L_LNST + (((l & 7) * 2 + h) * 4 + w) * 4) = v;
  }
  bar();
  STAMP(1);
  if (w == 0) {
    const v4f q = SM(v4f, L_MODRED + l * 16);
    const v2u g = {__float_as_uint((q.x + q.y) + (q.z + q.w)), tag.x};
    st64<SC1>(g, ws, b < 192 ? WS_MODP + (((b >> 5) * 4 + (b & 3)) * 512 + ((b >> 2) & 7) * 64 + l) * 8 : OOB, 0);
  }
  {
    const v4f q1 = SM(v4f, L_LNST + (l & 7) * 32), q2 = SM(v4f, L_LNST + (l & 7) * 32 + 16);
    const float mean = ((q1.x + q1.y) + (q1.z + q1.w)) * (1.f / 512);
    const float var = fmaxf(((q2.x + q2.y) + (q2.z + q2.w)) * (1.f / 512) - mean * mean, 0.f);
    const float rs = __builtin_amdgcn_rsqf(var + 1e-5f), mb = -mean * rs;
    UNROLL for (int i = 0; i < 2; ++i) {
      v4u o;
      UNROLL for (int k = 0; k < 4; ++k) o[k] = pk(fmaf(f_lo(xr[i][k]), rs, mb), fmaf(f_hi(xr[i][k]), rs, mb));
      st128<SC1>(o, ws, WS_XN + (b >> 2) * 32768 + (16 * w + (l >> 3)) * 512 + (8 * (b & 3) + (l & 7)) * 16, i * 4096);
    }
  }
  set_flag(ws, 0, b, w, l, tag);
  asm volatile("" ::"v"(pf));
  STAMP(2);
  UNROLL for (int i = 0; i < 8; ++i) wq[i] = ld128<0>(rqkv, wvo, i * 3072);
  const v4u wphi_raw = ld128<0>(rphi, w == 0 ? hp * 1024 + l * 16 : OOB, 0);
  pin();
  v2f sc[4], sa[4];
  {
    v2f ca2, sa2;
    for (u32 sp = 0; sp < SPIN_CAP; ++sp) {
      v4u g[8]; bool ok = true;
      UNROLL for (int i = 0; i < 8; ++i) g[i] = ld128<SC1>(ws, WS_MODP + w * 1024 + l * 16, i * 4096);
      UNROLL for (int i = 0; i < 8; ++i) ok &= (g[i].y == tag.x) & (g[i].w == tag.x);
      sa2 = (v2f){(__uint_as_float(g[0].x) + __uint_as_float(g[1].x)) + (__uint_as_float(g[2].x) + __uint_as_float(g[3].x)),
                  (__uint_as_float(g[0].z) + __uint_as_float(g[1].z)) + (__uint_as_float(g[2].z) + __uint_as_float(g[3].z))};
      ca2 = (v2f){(__uint_as_float(g[4].x) + __uint_as_float(g[5].x)) + (__uint_as_float(g[6].x) + __uint_as_float(g[7].x)),
                  (__uint_as_float(g[4].z) + __uint_as_float(g[5].z)) + (__uint_as_float(g[6].z) + __uint_as_float(g[7].z))};
      if (__all(ok)) break;
      asm volatile("" ::: "memory");
    }
    pin();
    const float c0 = rbf(1.f + rbf(ca2.x)), c1 = rbf(1.f + rbf(ca2.y)), a0 = rbf(sa2.x), a1 = rbf(sa2.y);
    sc[0] = (v2f){dpp<0x00>(c0), dpp<0x00>(c1)}; sc[1] = (v2f){dpp<0x55>(c0), dpp<0x55>(c1)};
    sc[2] = (v2f){dpp<0xAA>(c0), dpp<0xAA>(c1)}; sc[3] = (v2f){dpp<0xFF>(c0), dpp<0xFF>(c1)};
    sa[0] = (v2f){dpp<0x00>(a0), dpp<0x00>(a1)}; sa[1] = (v2f){dpp<0x55>(a0), dpp<0x55>(a1)};
    sa[2] = (v2f){dpp<0xAA>(a0), dpp<0xAA>(a1)}; sa[3] = (v2f){dpp<0xFF>(a0), dpp<0xFF>(a1)};
  }
  STAMP(3);
  wait_flags(ws, WS_FLAG + (16 * rg + 4 * w + (l >> 2)) * 128 + (l & 3) * 8, l < 16, tag);
  STAMP(4);
  v4u xf[32];
  UNROLL for (int s = 0; s < 32; ++s) xf[s] = ld128<SC1>(ws, WS_XN + (4 * rg + w) * 32768 + l * 16 + (s & 3) * 1024, (s >> 2) * 4096);
  pin();
  const u32 wc = l & 3, wg = l >> 2;
  const u32 wbase = (8 * w + (wg >> 1)) * 1024 + (wg & 1) * 512 + (4 * (wc >> 1) + 16 * (wc & 1)) * 16;
  const u32 wxor = (wc & 1) ^ (2 * (wg & 1));
  const u32 bt = ((l >> 3) & 1) + 2 * ((l >> 4) & 1) + 4 * (l >> 5);
  const u32 bias_dst = L_BIAS + (((bt & 3) + 4 * (wc >> 1) + 8 * (bt >> 2) + 16 * (wc & 1)) * 4 + w) * 4;
  {
    float bp[8];
    UNROLL for (int t = 0; t < 8; ++t) bp[t] = wcol(wk, t, sc, sa, sm + L_WK + wbase + (((t & 3) ^ wxor) + 8 * (t >> 2)) * 16);
    bias_red(bp, l, sm + bias_dst);
    UNROLL for (int t = 0; t < 8; ++t) bp[t] = wcol(wv, t, sc, sa, sm + L_WV + wbase + (((t & 3) ^ wxor) + 8 * (t >> 2)) * 16);
    bias_red(bp, l, sm + bias_dst + 512);
  }
  if (w == 0) SM(v4u, L_WPHI + l * 16) = wphi_raw;
  bar();
  STAMP(5);
  const u32 frd = h * 512 + (r ^ (r >> 4) ^ (2 * h)) * 16;
  const v4u ones = {0x3f803f80u, 0u, 0u, 0u};
  const v16f zero16 = {0, 0, 0, 0, 0, 0, 0, 0, 0, 0, 0, 0, 0, 0, 0, 0};
  v16f aK = mfma(bias_frag(sm + L_BIAS + r * 16, h), ones, zero16);
  v16f aV = mfma(ones, bias_frag(sm + L_BIAS + 512 + r * 16, h), zero16);
  {
    float bp[8];
    UNROLL for (int s = 0; s < 32; ++s) {
      aK = mfma(SM(v4u, L_WK + s * 1024 + frd), xf[s], aK);
      aV = mfma(xf[s], SM(v4u, L_WV + s * 1024 + frd), aV);
      if ((s & 3) == 0) { const int t = s >> 2; bp[t] = wcol(wq, t, sc, sa, sm + L_WQ + wbase + (((t & 3) ^ wxor) + 8 * (t >> 2)) * 16); }
    }
    bias_red(bp, l, sm + bias_dst + 1024);
  }
  STAMP(6);
  const u32 tq = (l >> 2) & 3, tp = l & 3, tG = (l >> 4) & 1;
  v4u phA[2];
  {
    const bool on = (tp & 1) == h;
    const u32 base = L_WPHI + h * 512 + tq * 32 + (2 * tG + (tp >> 1)) * 8;
    UNROLL for (int s = 0; s < 2; ++s) phA[s] = trd2(sm + (on ? base + s * 256 : L_ZERO), sm + (on ? base + s * 256 + 128 : L_ZERO));
  }
  const bool hd = (r >> 2) & 1;
  {
    v16f p = mfma(phA[0], pack8(aK, 0), zero16);
    p = mfma(phA[1], pack8(aK, 1), p);
    u32 fk[16];
    features(p, fk);
    const u32 scr = L_SCR + w * 4096;
    UNROLL for (int i = 0; i < 4; ++i) { v4u o = {fk[4 * i], fk[4 * i + 1], fk[4 * i + 2], fk[4 * i + 3]}; SM(v4u, scr + h * 2048 + r * 64 + ((i ^ ((r >> 1) & 3)) * 16)) = o; }
    v16f kv[2];
    UNROLL for (int e = 0; e < 2; ++e) {
      kv[e] = zero16;
      UNROLL for (int s = 0; s < 2; ++s) {
        const u32 a0 = scr + e * 2048 + (16 * s + 4 * h + tq) * 64 + (((2 * tG + (tp >> 1)) ^ (2 * h + (tq >> 1))) * 16) + (tp & 1) * 8;
        const v4u vf = pack8(aV, s);
        const bool mine = hd == (e == 1);
        const v4u bf = {mine ? vf.x : 0x3f803f80u, mine ? vf.y : 0x3f803f80u, mine ? vf.z : 0x3f803f80u, mine ? vf.w : 0x3f803f80u};
        kv[e] = mfma(trd2(sm + a0, sm + a0 + 512), bf, kv[e]);
      }
    }
    UNROLL for (int g = 0; g < 4; ++g) {
      v4f R, Z;
      UNROLL for (int i = 0; i < 4; ++i) { R[i] = hd ? kv[1][4 * g + i] : kv[0][4 * g + i]; Z[i] = hd ? kv[0][4 * g + i] : kv[1][4 * g + i]; }
      SM(v4f, scr + g * 1024 + l * 16) = R;
      if ((r & 27) == 0) SM(v4f, L_ZP + ((w * 4 + g) * 4 + (hd ? 0 : 2) + h) * 16) = Z;
    }
  }
  bar();
  {
    v4f s = SM(v4f, L_SCR + w * 1024 + l * 16), z = SM(v4f, L_ZP + (w * 4 + (l & 3)) * 16);
    UNROLL for (int i = 1; i < 4; ++i) { s += SM(v4f, L_SCR + i * 4096 + w * 1024 + l * 16); z += SM(v4f, L_ZP + ((i * 4 + w) * 4 + (l & 3)) * 16); }
    const u32 dst = WS_KVP + ((hp * 16 + rg) * 4 + w) * 1152;
    st128<SC1>(__builtin_bit_cast(v4u, s), ws, dst + l * 16, 0);
    st128<SC1>(__builtin_bit_cast(v4u, z), ws, l < 8 ? dst + 1024 + l * 16 : OOB, 0);
  }
  set_flag(ws, 1, rg * 16 + hp, w, l, tag);
  STAMP(7);
  {
    const v4u srd = {(u32)(u64)pw1, (u32)((u64)pw1 >> 32), 1409024u, 0x00020000u};
    const u32 vo = (128 * w + (l >> 2)) * 2752 + tau0 * 64 + (l & 3) * 16;
    UNROLL for (int i = 0; i < 8; ++i) UNROLL for (int t = 0; t < 3; ++t)
      dma16(srd, (t == 2 && cg >= 11) ? OOB : vo, i * 44032 + t * 64, (u32)(size_t)sm + L_W1 + t * 32768 + w * 8192 + i * 1024);
  }
  const u32 mo = w >> 1, qo = w & 1;
  const u32 wcolo = ct * 128 + (r ^ (16 * qo)) * 4;
  v4u woA[8][2];
  {
    u32 L[64];
    UNROLL for (int s = 0; s < 8; ++s) UNROLL for (int jj = 0; jj < 8; ++jj)
      L[s * 8 + jj] = ld32<0>(rwo, (128 * w + 16 * h) * 1024 + wcolo + (jj & 3) * 1024, (32 * (s >> 1) + 8 * (s & 1) + (jj & 4)) * 1024);
    pin();
    v16f aQ = mfma(bias_frag(sm + L_BIAS + 1024 + r * 16, h), ones, zero16);
    UNROLL for (int s = 0; s < 32; ++s) aQ = mfma(SM(v4u, L_WQ + s * 1024 + frd), xf[s], aQ);
    aK = mfma(phA[0], pack8(aQ, 0), zero16);
    aK = mfma(phA[1], pack8(aQ, 1), aK);
    UNROLL for (int s = 0; s < 8; ++s) UNROLL for (int i = 0; i < 4; ++i) {
      woA[s][0][i] = prm_lo(L[s * 8 + 2 * i], L[s * 8 + 2 * i + 1]);
      woA[s][1][i] = prm_hi(L[s * 8 + 2 * i], L[s * 8 + 2 * i + 1]);
    }
  }
  u32 fq[16];
  features(aK, fq);
  pin();
  STAMP(8);
  wait_flags(ws, WS_FLAG + (256 + l * 16 + hp) * 128 + w * 8, l < 16, tag);
  STAMP(9);
  {
    v4u P[16];
    const u32 src = WS_KVP + (hp * 64 + w) * 1152;
    UNROLL for (int i = 0; i < 16; ++i) P[i] = ld128<SC1>(ws, src + l * 16, i * 4608);
    const v4f zp = __builtin_bit_cast(v4f, ld128<SC1>(ws, src + 1024 + (l >> 2) * 4608 + (l & 3) * 16, 0));
    v4f s = __builtin_bit_cast(v4f, P[0]);
    UNROLL for (int i = 1; i < 16; ++i) s += __builtin_bit_cast(v4f, P[i]);
    const v2u o = {pk(s.x, s.y), pk(s.z, s.w)};
    SM(v2u, L_KVF + w * 512 + r * 16 + h * 8) = o;
    float e = swp16_add(swp32_add(zp.x, zp.z), swp32_add(zp.y, zp.w));
    e += dpp<0x128>(e); e += dpp<0x124>(e);
    SM(unsigned short, L_ZF + (((l >> 1) & 1) * 32 + 8 * w + 4 * (l & 1) + (l >> 4)) * 2) = (unsigned short)pk(e, e);
  }
  drain();
  bar();
  {
    const bool on = hd == (h == 1);
    v16f num = zero16, den = zero16;
    UNROLL for (int s = 0; s < 4; ++s) {
      const v4u bq = {fq[4 * s], fq[4 * s + 1], fq[4 * s + 2], fq[4 * s + 3]};
      num = mfma(SM(v4u, on ? L_KVF + s * 512 + r * 16 : L_ZERO), bq, num);
      den = mfma(SM(v4u, on ? L_ZF + h * 64 + s * 16 : L_ZERO), bq, den);
    }
    const float rd = __builtin_amdgcn_rcpf(den[0] + 1e-6f);
    num *= rd;
    UNROLL for (int s = 0; s < 2; ++s) st128<SC1>(pack8(num, s), ws, WS_O + (4 * rg + w) * 32768 + (2 * hp) * 1024 + l * 16 + s * 1024, 0);
  }
  set_flag(ws, 2, rg * 16 + hp, w, l, tag);
  STAMP(10);
  v4u xres[2];
  UNROLL for (int g = 0; g < 2; ++g) xres[g] = ld128<0>(rx, (64 * rt + 32 * mo + r) * 1024 + ct * 128 + qo * 64 + h * 16 + g * 32, 0);
  {
    u32 m2[4];
    for (u32 sp = 0; sp < SPIN_CAP; ++sp) {
      bool ok = true;
      UNROLL for (int p = 0; p < 4; ++p) {
        v4u g[4];
        UNROLL for (int i = 0; i < 4; ++i) { g[i] = ld128<SC1>(ws, WS_MODP + w * 1024 + l * 16, (8 + 4 * p + i) * 4096); ok &= (g[i].y == tag.x) & (g[i].w == tag.x); }
        float a = (__uint_as_float(g[0].x) + __uint_as_float(g[1].x)) + (__uint_as_float(g[2].x) + __uint_as_float(g[3].x));
        float c = (__uint_as_float(g[0].z) + __uint_as_float(g[1].z)) + (__uint_as_float(g[2].z) + __uint_as_float(g[3].z));
        if (p == 2) { a = 1.f + rbf(a); c = 1.f + rbf(c); }
        m2[p] = pk(a, c);
      }
      if (__all(ok)) break;
      asm volatile("" ::: "memory");
    }
    pin();
    UNROLL for (int p = 0; p < 4; ++p) SM(u32, L_MOD2 + p * 1024 + w * 256 + l * 4) = m2[p];
  }
  bar();
  const u32 w1rd = (8 * h + tq) * 64 + (16 * tG + 4 * tp) * 2;
  if (w < 3) {
    v16f cs = zero16;
    UNROLL for (int s = 0; s < 32; ++s) {
      LDS char* p = sm + L_W1 + w * 32768 + s * 1024 + w1rd;
      cs = mfma(SM(v4u, L_MOD2 + ((r & 1) ? 1024 : 2048) + s * 32 + h * 16), trd2(p, p + 256), cs);
    }
    const float ch = rbf(cs[0]), bh = rbf(cs[1]);
    const v4u f = {pk(ch, cs[0] - ch), pk(ch, bh), pk(cs[1] - bh, bh), 0u};
    const v4u z4 = {0u, 0u, 0u, 0u};
    SM(v4u, L_CF + w * 1024 + l * 16) = h ? z4 : f;
  }
  pin();
  STAMP(11);
  wait_flags(ws, WS_FLAG + (512 + (rt >> 1) * 16 + 4 * w + (l >> 1)) * 128 + (2 * (rt & 1) + (l & 1)) * 8, l < 8, tag);
  STAMP(12);
  u32 x1p[8];
  {
    v4u of[2][8];
    UNROLL for (int m = 0; m < 2; ++m) UNROLL for (int s = 0; s < 8; ++s)
      of[m][s] = ld128<SC1>(ws, WS_O + (2 * rt + (mo ^ 1 ^ m)) * 32768 + w * 8192 + l * 16 + (s & 3) * 1024, (s >> 2) * 4096);
    pin();
    v16f a0 = zero16, a1 = zero16;
    UNROLL for (int s = 0; s < 8; ++s) { a0 = mfma(woA[s][0], of[0][s], a0); a1 = mfma(woA[s][1], of[0][s], a1); }
    v16f c0 = zero16, c1 = zero16;
    UNROLL for (int s = 0; s < 8; ++s) { c0 = mfma(woA[s][0], of[1][s], c0); c1 = mfma(woA[s][1], of[1][s], c1); }
    UNROLL for (int g = 0; g < 4; ++g) {
      const u32 d = L_RED + (((w ^ (2 + (g >> 1))) * 3 + 1 + (g >> 1)) * 4 + (g & 1) * 2) * 1024 + l * 16;
      SM(v4f, d) = (v4f){a0[4 * g], a0[4 * g + 1], a0[4 * g + 2], a0[4 * g + 3]};
      SM(v4f, d + 1024) = (v4f){a1[4 * g], a1[4 * g + 1], a1[4 * g + 2], a1[4 * g + 3]};
    }
    UNROLL for (int g = 2; g < 4; ++g) {
      const u32 d = L_RED + (((w ^ 1) * 3) * 4 + (g & 1) * 2) * 1024 + l * 16;
      SM(v4f, d) = (v4f){c0[4 * g], c0[4 * g + 1], c0[4 * g + 2], c0[4 * g + 3]};
      SM(v4f, d + 1024) = (v4f){c1[4 * g], c1[4 * g + 1], c1[4 * g + 2], c1[4 * g + 3]};
    }
    bar();
    float s1 = 0.f, s2 = 0.f;
    UNROLL for (int g = 0; g < 2; ++g) {
      v4f t0 = {c0[4 * g], c0[4 * g + 1], c0[4 * g + 2], c0[4 * g + 3]}, t1 = {c1[4 * g], c1[4 * g + 1], c1[4 * g + 2], c1[4 * g + 3]};
      UNROLL for (int k = 0; k < 3; ++k) { const u32 d = L_RED + ((w * 3 + k) * 4 + g * 2) * 1024 + l * 16; t0 += SM(v4f, d); t1 += SM(v4f, d + 1024); }
      const u32 mo2 = L_MOD2 + ct * 128 + qo * 64 + g * 32 + h * 16;
      const v4u ga = SM(v4u, mo2), cb = SM(v4u, mo2 + 2048);
      v4u u;
      UNROLL for (int i = 0; i < 4; ++i) {
        const u32 x1 = pk(fmaf(f_lo(ga[i]), t0[i], f_lo(xres[g][i])), fmaf(f_hi(ga[i]), t1[i], f_hi(xres[g][i])));
        x1p[4 * g + i] = x1;
        u[i] = pk(f_lo(x1) * f_lo(cb[i]), f_hi(x1) * f_hi(cb[i]));
        s1 = dot2(x1, 0x3f803f80u, s1); s2 = dot2(x1, x1, s2);
      }
      st128<SC1>(u, ws, WS_U + (2 * rt + mo) * 32768 + (4 * ct + 2 * qo + g) * 1024 + l * 16, 0);
    }
    const v2u st = {__float_as_uint(swp32_add(s1, s1)), __float_as_uint(swp32_add(s2, s2))};
    st64<SC1>(st, ws, h ? OOB : WS_ST + (((2 * rt + mo) * 16 + 2 * ct + qo) * 32 + r) * 8, 0);
  }
  set_flag(ws, 3, rt * 8 + ct, w, l, tag);
  STAMP(13);
  v4u w2A[22][2];
  UNROLL for (int s = 0; s < 22; ++s) {
    u32 L[8];
    UNROLL for (int jj = 0; jj < 8; ++jj) L[jj] = ld32<0>(rw2, (352 * w + 4 * h) * 1024 + wcolo + (jj & 3) * 1024, (16 * s + 2 * (jj & 4)) * 1024);
    UNROLL for (int i = 0; i < 4; ++i) { w2A[s][0][i] = prm_lo(L[2 * i], L[2 * i + 1]); w2A[s][1][i] = prm_hi(L[2 * i], L[2 * i + 1]); }
  }
  UNROLL for (int s = 0; s < 22; ++s) asm volatile("" : "+a"(w2A[s][0]), "+a"(w2A[s][1]));
  pin();
  STAMP(14);
  {
    const u32 T = 4 * rg2 + w;
    wait_flags(ws, WS_FLAG + (768 + (T >> 1) * 8 + (l >> 1)) * 128 + (2 * (T & 1) + (l & 1)) * 8, l < 16, tag);
    STAMP(15);
    UNROLL for (int s = 0; s < 32; ++s) xf[s] = ld128<SC1>(ws, WS_U + T * 32768 + l * 16 + (s & 3) * 1024, (s >> 2) * 4096);
    v2u sp[8];
    UNROLL for (int i = 0; i < 8; ++i) sp[i] = ld64<SC1>(ws, WS_ST + T * 4096 + h * 2048 + r * 8 + i * 256, 0);
    pin();
    v16f a[3] = {zero16, zero16, zero16};
    UNROLL for (int s = 0; s < 32; ++s) UNROLL for (int t = 0; t < 3; ++t) {
      LDS char* p = sm + L_W1 + t * 32768 + s * 1024 + w1rd;
      a[t] = mfma(trd2(p, p + 256), xf[s], a[t]);
    }
    v2f ss = __builtin_bit_cast(v2f, sp[0]);
    UNROLL for (int i = 1; i < 8; ++i) ss += __builtin_bit_cast(v2f, sp[i]);
    const float mean = swp32_add(ss.x, ss.x) * (1.f / 512);
    const float var = fmaxf(swp32_add(ss.y, ss.y) * (1.f / 512) - mean * mean, 0.f) + 1e-5f;
    const float rs = __builtin_amdgcn_rsqf(var), sd = var * rs;
    const float mh = rbf(-mean), sh = rbf(sd);
    const v4u cb = {pk(mh, mh), pk(-mean - mh, sh), pk(sh, sd - sh), 0u};
    UNROLL for (int t = 0; t < 3; ++t) {
      a[t] = mfma(SM(v4u, L_CF + t * 1024 + l * 16), cb, a[t]);
      a[t] *= rs;
      UNROLL for (int s = 0; s < 2; ++s) {
        v4u o = pack8(a[t], s);
        UNROLL for (int i = 0; i < 4; ++i) o[i] = relu2(o[i]);
        st128<SC1>(o, ws, (t == 2 && cg >= 11) ? OOB : WS_F + (T * 86 + 2 * tau0) * 1024 + l * 16, (2 * t + s) * 1024);
      }
    }
  }
  set_flag(ws, 4, rg2 * 16 + cg, w, l, tag);
  STAMP(16);
  {
    const u32 cg0 = w == 0 ? 0 : 4 * w - 1;
    wait_flags(ws, WS_FLAG + (1024 + (rt >> 1) * 16 + cg0 + (l >> 1)) * 128 + (2 * (rt & 1) + (l & 1)) * 8, l < 10, tag);
    STAMP(17);
    v4u ff[2][22];
    UNROLL for (int m = 0; m < 2; ++m) UNROLL for (int s = 0; s < 22; ++s) {
      const u32 vo = WS_F + ((2 * rt + (mo ^ 1 ^ m)) * 86 + 22 * w) * 1024 + l * 16;
      ff[m][s] = ld128<SC1>(ws, ((s >= 20 && w == 3) ? OOB : vo) + (s & 3) * 1024, (s >> 2) * 4096);
    }
    pin();
    v16f a0 = zero16, a1 = zero16;
    UNROLL for (int s = 0; s < 22; ++s) { a0 = mfma(w2A[s][0], ff[0][s], a0); a1 = mfma(w2A[s][1], ff[0][s], a1); }
    v16f c0 = zero16, c1 = zero16;
    UNROLL for (int s = 0; s < 22; ++s) { c0 = mfma(w2A[s][0], ff[1][s], c0); c1 = mfma(w2A[s][1], ff[1][s], c1); }
    UNROLL for (int g = 0; g < 4; ++g) {
      const u32 d = L_RED + (((w ^ (2 + (g >> 1))) * 3 + 1 + (g >> 1)) * 4 + (g & 1) * 2) * 1024 + l * 16;
      SM(v4f, d) = (v4f){a0[4 * g], a0[4 * g + 1], a0[4 * g + 2], a0[4 * g + 3]};
      SM(v4f, d + 1024) = (v4f){a1[4 * g], a1[4 * g + 1], a1[4 * g + 2], a1[4 * g + 3]};
    }
    UNROLL for (int g = 2; g < 4; ++g) {
      const u32 d = L_RED + (((w ^ 1) * 3) * 4 + (g & 1) * 2) * 1024 + l * 16;
      SM(v4f, d) = (v4f){c0[4 * g], c0[4 * g + 1], c0[4 * g + 2], c0[4 * g + 3]};
      SM(v4f, d + 1024) = (v4f){c1[4 * g], c1[4 * g + 1], c1[4 * g + 2], c1[4 * g + 3]};
    }
    bar();
    UNROLL for (int g = 0; g < 2; ++g) {
      v4f t0 = {c0[4 * g], c0[4 * g + 1], c0[4 * g + 2], c0[4 * g + 3]}, t1 = {c1[4 * g], c1[4 * g + 1], c1[4 * g + 2], c1[4 * g + 3]};
      UNROLL for (int k = 0; k < 3; ++k) { const u32 d = L_RED + ((w * 3 + k) * 4 + g * 2) * 1024 + l * 16; t0 += SM(v4f, d); t1 += SM(v4f, d + 1024); }
      const v4u gb = SM(v4u, L_MOD2 + 3072 + ct * 128 + qo * 64 + g * 32 + h * 16);
      v4u o;
      UNROLL for (int i = 0; i < 4; ++i) o[i] = pk(fmaf(f_lo(gb[i]), t0[i], f_lo(x1p[4 * g + i])), fmaf(f_hi(gb[i]), t1[i], f_hi(x1p[4 * g + i])));
      st128<0>(o, rout, (64 * rt + 32 * mo + r) * 1024 + ct * 128 + qo * 64 + h * 16 + g * 32, 0);
    }
  }
  st64<SC1>(tag, ws, (b == 0 && tid == 0) ? WS_NONCE : OOB, 0);
  STAMP(18);
  asm volatile(".Lpf_end:");
}

extern "C" void kernel_launch(void* const* d_in, const int* in_sizes, int n_in, void* d_out, int out_size, void* d_ws, size_t ws_size,
                              hipStream_t stream) {
  fused_block<<<256, 256, 0, stream>>>(d_in[0], d_in[1], d_in[2], d_in[3], d_in[5], d_in[6], d_in[7], d_in[8], d_out, (char*)d_ws);
}
```

```cpp
#include <hip/hip_runtime.h>

__global__ void __launch_bounds__(256, 1) fused_block(const void* p_c, unsigned n_c, unsigned f_c, const void* p_wmod, unsigned n_wmod, unsigned f_wmod, const void* p_x, unsigned n_x, unsigned f_x, const void* p_ws, unsigned n_ws, unsigned f_ws, const void* p_wqkv, unsigned n_wqkv, unsigned f_wqkv, const void* p_wphi, unsigned n_wphi, unsigned f_wphi, const void* p_wo, unsigned n_wo, unsigned f_wo, const void* p_w1, unsigned n_w1, unsigned f_w1, const void* p_w2, unsigned n_w2, unsigned f_w2, const void* p_out, unsigned n_out, unsigned f_out) {
  __shared__ __attribute__((aligned(16))) char smem[163840];
  unsigned tb = threadIdx.x | (blockIdx.x << 8) | (unsigned)(size_t)(__attribute__((address_space(3))) char*)smem;
  asm volatile(
    "s_getpc_b64 s[8:9]\n"
    ".Lpf_here:\n"
    "s_load_dwordx16 s[16:31], %1, 0\n"
    "s_load_dwordx16 s[32:47], %1, 64\n"
    "s_load_dwordx8 s[48:55], %1, 0x80\n"
    "s_mov_b32 s10, .Lpf_end-.Lpf_here\n"
    "s_mov_b32 s11, 0x20000\n"
    "v_bfe_u32 v28, %0, 11, 3\n"
    "v_bfe_u32 v10, %0, 6, 2\n"
    "v_lshl_or_b32 v28, v28, 2, v10\n"
    "v_and_b32 v10, 7, %0\n"
    "v_lshl_or_b32 v10, v28, 3, v10\n"
    "v_lshlrev_b32 v10, 7, v10\n"
    "s_mov_b64 exec, 0xff\n"
    "buffer_load_dword v29, v10, s[8:11], 0 offen\n"
    "s_mov_b64 exec, -1\n"
    "v_and_b32 v1, 63, %0\n"
    "v_readfirstlane_b32 s56, %0\n"
    "s_bfe_u32 s15, s56, 0x20006\n"
    "s_lshr_b32 s56, s56, 8\n"
    "v_lshrrev_b32 v51, 3, v1\n"
    "v_and_b32 v52, 7, v1\n"
    "v_lshlrev_b32 v48, 3, v51\n"
    "s_and_b32 s4, s56, 3\n"
    "s_lshl_b32 s4, s4, 2\n"
    "s_add_u32 s4, s4, s15\n"
    "s_lshl_b32 s5, s4, 6\n"
    "s_lshr_b32 s6, s56, 2\n"
    "s_mul_i32 s95, s4, 0x30000\n"
    "s_lshl_b32 s7, s6, 7\n"
    "s_add_u32 s95, s95, s7\n"
    "s_cmp_lt_u32 s56, 64\n"
    "s_cselect_b32 s90, 0, 0x80000000\n"
    "v_mul_u32_u24 v255, 0x6000, v51\n"
    "v_lshl_or_b32 v255, v52, 4, v255\n"
    "v_or_b32 v49, s90, v255\n"
    "v_lshlrev_b32 v2, 4, v1\n"
    "s_bfe_u32 s60, s56, 0x30003\n"
    "s_and_b32 s61, s56, 1\n"
    "s_lshl_b32 s58, s61, 3\n"
    "s_add_u32 s58, s58, s60\n"
    "s_bfe_u32 s57, s56, 0x20001\n"
    "s_lshr_b32 s4, s56, 6\n"
    "s_lshl_b32 s57, s57, 2\n"
    "s_add_u32 s57, s57, s4\n"
    "s_lshr_b32 s4, s60, 2\n"
    "s_lshl_b32 s59, s57, 1\n"
    "s_add_u32 s59, s59, s4\n"
    "s_and_b32 s60, s60, 3\n"
    "s_lshl_b32 s60, s60, 0\n"
    "s_lshl_b32 s7, s61, 2\n"
    "s_add_u32 s60, s60, s7\n"
    "s_lshl_b32 s96, s59, 3\n"
    "s_add_u32 s96, s96, s60\n"
    "s_lshl_b32 s4, s96, 13\n"
    "s_lshl_b32 s7, s15, 11\n"
    "s_add_u32 s4, s4, s7\n"
    "s_waitcnt lgkmcnt(0)\n"
    "buffer_load_dwordx2 v[54:55], v48, s[16:19], s5 offen\n"
    "s_cmp_eq_u32 s90, 0\n"
    "s_cselect_b64 exec, -1, 1\n"
    "buffer_load_dwordx4 v[128:131], v49, s[20:23], s95 offen\n"
    "s_add_u32 s6, s95, 0x1800\n"
    "buffer_load_dwordx4 v[132:135], v49, s[20:23], s6 offen\n"
    "s_add_u32 s6, s95, 0x3000\n"
    "buffer_load_dwordx4 v[136:139], v49, s[20:23], s6 offen\n"
    "s_add_u32 s6, s95, 0x4800\n"
    "buffer_load_dwordx4 v[140:143], v49, s[20:23], s6 offen\n"
    "s_mov_b64 exec, -1\n"
    "buffer_load_dwordx4 v[56:59], v2, s[24:27], s4 offen\n"
    "buffer_load_dwordx4 v[60:63], v2, s[24:27], s4 offen offset:1024\n"
    "s_mov_b64 exec, 0x1\n"
    "buffer_load_dwordx2 v[6:7], off, s[28:31], 0 sc1\n"
    "s_mov_b64 exec, -1\n"
    "s_lshr_b32 s2, s15, 1\n"
    "s_and_b32 s3, s15, 1\n"
    "v_and_b32 v4, 31, v1\n"
    "v_lshrrev_b32 v5, 5, v1\n"
    "v_lshlrev_b32 v11, 10, v4\n"
    "v_lshl_or_b32 v11, v5, 4, v11\n"
    "s_lshl_b32 s86, s59, 16\n"
    "s_lshl_b32 s7, s2, 15\n"
    "s_add_u32 s86, s86, s7\n"
    "s_lshl_b32 s7, s60, 7\n"
    "s_add_u32 s86, s86, s7\n"
    "s_lshl_b32 s7, s3, 6\n"
    "s_add_u32 s86, s86, s7\n"
    "s_lshl_b32 s88, s58, 10\n"
    "s_lshl_b32 s7, s15, 8\n"
    "s_add_u32 s88, s88, s7\n"
    "s_mov_b64 exec, 0xffff\n"
    "buffer_load_dwordx4 v[168:171], v2, s[36:39], s88 offen\n"
    "s_mov_b64 exec, -1\n"
    "s_mul_i32 s88, s56, 0x1800\n"
    "s_mul_i32 s7, s15, 0x600\n"
    "s_add_u32 s88, s88, s7\n"
    "v_lshlrev_b32 v31, 7, v1\n"
    "s_mov_b64 exec, 0xfff\n"
    "buffer_load_dword v30, v31, s[32:35], s88 offen sc1\n"
    "s_mov_b64 exec, -1\n"
    "s_mov_b32 s72, 0x5040100\n"
    "s_mov_b32 s73, 0x7060302\n"
    "s_mov_b32 s74, 0x3fb8aa3b\n"
    "s_mov_b32 s75, 0xffff0000\n"
    "s_mov_b32 s14, 0x100000\n"
    "v_lshlrev_b32 v3, 3, v1\n"
    "s_lshl_b32 s4, s15, 2\n"
    "v_lshlrev_b32 v254, 7, v52\n"
    "v_lshl_or_b32 v254, v51, 4, v254\n"
    "v_add_u32 v254, s4, v254\n"
    "v_lshrrev_b32 v50, 4, v1\n"
    "s_lshl_b32 s4, s15, 6\n"
    "v_lshl_add_u32 v50, v50, 4, s4\n"
    "v_or_b32 v53, s90, v3\n"
    "s_lshr_b32 s4, s56, 5\n"
    "s_and_b32 s5, s56, 3\n"
    "s_bfe_u32 s6, s56, 0x30002\n"
    "buffer_load_dwordx4 v[172:175], v11, s[24:27], s86 offen\n"
    "s_lshl_b32 s90, s4, 14\n"
    "s_lshl_b32 s7, s5, 12\n"
    "s_add_u32 s90, s90, s7\n"
    "s_lshl_b32 s7, s6, 9\n"
    "s_add_u32 s90, s90, s7\n"
    "s_add_u32 s90, s90, 0x40000\n"
    "s_lshl_b32 s96, s96, 7\n"
    "s_add_u32 s96, s96, 0x38000\n"
    "v_mov_b32 v10, 0x80000000\n"
    "s_cmp_eq_u32 s15, 0\n"
    "s_cselect_b32 s96, s90, s96\n"
    "s_cselect_b64 vcc, -1, 0\n"
    "s_cselect_b32 s5, 0, 0x400\n"
    "v_cndmask_b32 v53, v10, v53, vcc\n"
    "v_cndmask_b32 v253, v10, v3, vcc\n"
    "v_add_u32 v49, s5, v2\n"
    "s_cmp_eq_u32 s15, 1\n"
    "s_cselect_b32 vcc_lo, 0xffff, 0\n"
    "s_mov_b32 vcc_hi, 0\n"
    "v_cndmask_b32 v53, v53, v3, vcc\n"
    "s_sub_u32 s4, s56, 64\n"
    "s_cmp_lt_u32 s4, 0x80\n"
    "s_cselect_b32 s4, 0, 0x80000000\n"
    "v_or_b32 v255, s4, v255\n"
    "v_or_b32 v253, s4, v253\n"
    "s_lshl_b32 s4, s59, 2\n"
    "s_add_u32 s4, s4, s15\n"
    "s_lshl_b32 s67, s60, 18\n"
    "s_lshl_b32 s7, s4, 11\n"
    "s_add_u32 s67, s67, s7\n"
    "s_add_u32 s67, s67, 0xe00000\n"
    "s_sub_u32 s5, s4, 32\n"
    "s_lshr_b32 s6, s5, 2\n"
    "s_min_u32 s6, s6, 11\n"
    "s_lshl_b32 s6, s6, 1\n"
    "s_add_u32 s6, s6, s5\n"
    "s_and_b32 s91, s5, 0x7e\n"
    "s_lshl_b32 s91, s91, 1\n"
    "s_add_u32 s91, s91, s5\n"
    "s_add_u32 s91, s91, 0xffffff44\n"
    "s_cmp_lt_u32 s5, 64\n"
    "s_cselect_b32 s5, s6, s91\n"
    "s_lshl_b32 s91, s5, 14\n"
    "s_lshl_b32 s7, s60, 7\n"
    "s_add_u32 s91, s91, s7\n"
    "s_lshr_b32 s5, s4, 1\n"
    "s_and_b32 s6, s4, 1\n"
    "s_lshl_b32 s92, s5, 15\n"
    "s_lshl_b32 s7, s6, 13\n"
    "s_add_u32 s92, s92, s7\n"
    "s_lshl_b32 s7, s60, 7\n"
    "s_add_u32 s92, s92, s7\n"
    "s_cmp_lt_u32 s4, 32\n"
    "s_cselect_b32 s91, s92, s91\n"
    "s_cselect_b64 s[8:9], s[40:41], s[48:49]\n"
    "s_cselect_b32 s10, s42, s50\n"
    "s_cselect_b32 s6, 1, 2\n"
    "s_cselect_b32 s5, 14, 12\n"
    "s_lshl_b32 s6, s6, 12\n"
    "s_add_u32 s92, s91, s6\n"
    "v_lshlrev_b32 v252, s5, v5\n"
    "v_lshl_or_b32 v252, v4, 2, v252\n"
    "s_cmp_lt_u32 s4, 0x76\n"
    "s_cselect_b32 s5, 0, 0x80000000\n"
    "buffer_load_dwordx4 v[176:179], v11, s[24:27], s86 offen offset:32\n"
    "v_or_b32 v252, s5, v252\n"
    "v_mov_b32 v148, 0\n"
    "v_mov_b32 v149, 0\n"
    "v_mov_b32 v150, 0\n"
    "v_mov_b32 v151, 0\n"
    "s_mov_b32 s5, 0x3f803f80\n"
    "s_waitcnt vmcnt(11)\n"
    "v_lshlrev_b32 v152, 16, v54\n"
    "v_and_b32 v153, s75, v54\n"
    "v_lshlrev_b32 v154, 16, v55\n"
    "v_and_b32 v155, s75, v55\n"
    "v_mul_f32 v156, 0xbfb8aa3b, v152\n"
    "v_mul_f32 v157, 0xbfb8aa3b, v153\n"
    "v_mul_f32 v158, 0xbfb8aa3b, v154\n"
    "v_mul_f32 v159, 0xbfb8aa3b, v155\n"
    "v_exp_f32 v156, v156\n"
    "v_exp_f32 v157, v157\n"
    "v_exp_f32 v158, v158\n"
    "v_exp_f32 v159, v159\n"
    "v_add_f32 v156, 1.0, v156\n"
    "v_add_f32 v157, 1.0, v157\n"
    "v_add_f32 v158, 1.0, v158\n"
    "v_add_f32 v159, 1.0, v159\n"
    "v_rcp_f32 v156, v156\n"
    "v_rcp_f32 v157, v157\n"
    "v_rcp_f32 v158, v158\n"
    "v_rcp_f32 v159, v159\n"
    "v_pk_mul_f32 v[152:153], v[152:153], v[156:157]\n"
    "v_pk_mul_f32 v[154:155], v[154:155], v[158:159]\n"
    "v_cvt_pk_bf16_f32 v110, v152, v153\n"
    "v_cvt_pk_bf16_f32 v111, v154, v155\n"
    "s_sub_u32 s4, s56, 64\n"
    "s_cmp_lt_u32 s4, 0x80\n"
    "s_cselect_b64 exec, -1, 1\n"
    "buffer_load_dwordx4 v[180:183], v255, s[20:23], s95 offen\n"
    "s_add_u32 s6, s95, 0x1800\n"
    "buffer_load_dwordx4 v[184:187], v255, s[20:23], s6 offen\n"
    "s_add_u32 s6, s95, 0x3000\n"
    "buffer_load_dwordx4 v[188:191], v255, s[20:23], s6 offen\n"
    "s_add_u32 s6, s95, 0x4800\n"
    "buffer_load_dwordx4 v[192:195], v255, s[20:23], s6 offen\n"
    "s_mov_b64 exec, -1\n"
    "s_waitcnt vmcnt(13)\n"
    "v_perm_b32 v144, v132, v128, s72\n"
    "v_perm_b32 v145, v132, v128, s73\n"
    "v_dot2_f32_bf16 v160, v144, v110, 0\n"
    "v_dot2_f32_bf16 v161, v145, v110, 0\n"
    "v_perm_b32 v146, v133, v129, s72\n"
    "v_perm_b32 v147, v133, v129, s73\n"
    "v_dot2_f32_bf16 v162, v146, v110, 0\n"
    "v_dot2_f32_bf16 v163, v147, v110, 0\n"
    "v_perm_b32 v144, v134, v130, s72\n"
    "v_perm_b32 v145, v134, v130, s73\n"
    "v_dot2_f32_bf16 v164, v144, v110, 0\n"
    "v_dot2_f32_bf16 v165, v145, v110, 0\n"
    "v_perm_b32 v146, v135, v131, s72\n"
    "v_perm_b32 v147, v135, v131, s73\n"
    "v_dot2_f32_bf16 v166, v146, v110, 0\n"
    "v_dot2_f32_bf16 v167, v147, v110, 0\n"
    "s_waitcnt vmcnt(11)\n"
    "v_perm_b32 v144, v140, v136, s72\n"
    "v_perm_b32 v145, v140, v136, s73\n"
    "v_dot2_f32_bf16 v160, v144, v111, v160\n"
    "v_dot2_f32_bf16 v161, v145, v111, v161\n"
    "v_perm_b32 v146, v141, v137, s72\n"
    "v_perm_b32 v147, v141, v137, s73\n"
    "v_dot2_f32_bf16 v162, v146, v111, v162\n"
    "v_dot2_f32_bf16 v163, v147, v111, v163\n"
    "v_perm_b32 v144, v142, v138, s72\n"
    "v_perm_b32 v145, v142, v138, s73\n"
    "v_dot2_f32_bf16 v164, v144, v111, v164\n"
    "v_dot2_f32_bf16 v165, v145, v111, v165\n"
    "v_perm_b32 v146, v143, v139, s72\n"
    "v_perm_b32 v147, v143, v139, s73\n"
    "v_dot2_f32_bf16 v166, v146, v111, v166\n"
    "v_dot2_f32_bf16 v167, v147, v111, v167\n"
    "s_waitcnt vmcnt(10)\n"
    "v_dot2c_f32_bf16 v148, s5, v56\n"
    "v_dot2c_f32_bf16 v149, v56, v56\n"
    "s_waitcnt vmcnt(9)\n"
    "v_dot2c_f32_bf16 v150, s5, v60\n"
    "v_dot2c_f32_bf16 v151, v60, v60\n"
    "v_dot2c_f32_bf16 v148, s5, v57\n"
    "v_dot2c_f32_bf16 v149, v57, v57\n"
    "v_dot2c_f32_bf16 v150, s5, v61\n"
    "v_dot2c_f32_bf16 v151, v61, v61\n"
    "v_dot2c_f32_bf16 v148, s5, v58\n"
    "v_dot2c_f32_bf16 v149, v58, v58\n"
    "v_dot2c_f32_bf16 v150, s5, v62\n"
    "v_dot2c_f32_bf16 v151, v62, v62\n"
    "v_dot2c_f32_bf16 v148, s5, v59\n"
    "v_dot2c_f32_bf16 v149, v59, v59\n"
    "v_dot2c_f32_bf16 v150, s5, v63\n"
    "v_dot2c_f32_bf16 v151, v63, v63\n"
    "v_permlane32_swap_b32 v160, v164\n"
    "v_permlane32_swap_b32 v161, v165\n"
    "v_permlane32_swap_b32 v162, v166\n"
    "v_permlane32_swap_b32 v163, v167\n"
    "v_permlane32_swap_b32 v148, v150\n"
    "v_permlane32_swap_b32 v149, v151\n"
    "v_add_f32 v160, v160, v164\n"
    "v_add_f32 v161, v161, v165\n"
    "v_add_f32 v162, v162, v166\n"
    "v_add_f32 v163, v163, v167\n"
    "v_add_f32 v148, v148, v150\n"
    "v_add_f32 v149, v149, v151\n"
    "v_permlane16_swap_b32 v160, v162\n"
    "v_permlane16_swap_b32 v161, v163\n"
    "v_mov_b32 v150, 0\n"
    "v_permlane16_swap_b32 v148, v149\n"
    "v_add_f32 v160, v160, v162\n"
    "v_add_f32 v161, v161, v163\n"
    "v_add_f32 v148, v148, v149\n"
    "v_mov_b32 v149, 0\n"
    "v_mov_b32 v151, 0\n"
    "v_add_f32_dpp v148, v148, v148 row_ror:8 row_mask:0xf bank_mask:0xf\n"
    "v_add_f32_dpp v160, v160, v160 row_ror:8 row_mask:0xf bank_mask:0xf\n"
    "v_add_f32_dpp v161, v161, v161 row_ror:8 row_mask:0xf bank_mask:0xf\n"
    "v_add_f32_dpp v148, v148, v148 row_ror:4 row_mask:0xf bank_mask:0xf\n"
    "s_mov_b32 vcc_lo, 0xff00ff00\n"
    "s_mov_b32 vcc_hi, 0xff00ff00\n"
    "v_cndmask_b32 v160, v160, v161, vcc\n"
    "v_add_f32_dpp v148, v148, v148 row_ror:2 row_mask:0xf bank_mask:0xf\n"
    "ds_write_b32 v254, v160\n"
    "s_nop 0\n"
    "v_add_f32_dpp v148, v148, v148 row_ror:1 row_mask:0xf bank_mask:0xf\n"
    "ds_write_b128 v50, v[148:151] offset:1024\n"
    "s_waitcnt lgkmcnt(0)\n"
    "s_barrier\n"
    "ds_read_b128 v[152:155], v49\n"
    "s_waitcnt vmcnt(8)\n"
    "v_readfirstlane_b32 s12, v6\n"
    "v_readfirstlane_b32 s13, v7\n"
    "s_add_u32 s12, s12, 1\n"
    "s_addc_u32 s13, s13, 0\n"
    "v_mov_b32 v6, s12\n"
    "v_mov_b32 v7, s13\n"
    "s_waitcnt lgkmcnt(0)\n"
    "v_pk_add_f32 v[152:153], v[152:153], v[154:155]\n"
    "v_add_f32 v152, v152, v153\n"
    "v_mov_b32 v153, s12\n"
    "buffer_store_dwordx2 v[152:153], v53, s[28:31], s96 offen sc1\n"
    "s_lshl_b32 s88, s57, 13\n"
    "s_lshl_b32 s7, s58, 9\n"
    "s_add_u32 s88, s88, s7\n"
    "s_add_u32 s88, s88, 0x1000000\n"
    "v_lshlrev_b32 v31, 17, v1\n"
    "s_mov_b64 exec, 0x3f\n"
    "buffer_load_dword v30, v31, s[28:31], s88 offen sc1\n"
    "s_mov_b64 exec, -1\n"
    "s_lshl_b32 s88, s60, 14\n"
    "s_lshl_b32 s7, s59, 9\n"
    "s_add_u32 s88, s88, s7\n"
    "s_add_u32 s88, s88, 0x1020000\n"
    "v_lshlrev_b32 v31, 7, v1\n"
    "s_mov_b64 exec, 0x3\n"
    "buffer_load_dword v30, v31, s[28:31], s88 offen sc1\n"
    "s_mov_b64 exec, -1\n"
    "v_lshrrev_b32 v28, 2, v1\n"
    "v_and_b32 v27, 3, v1\n"
    "v_mul_u32_u24 v251, 0xc00, v28\n"
    "v_lshl_or_b32 v251, v27, 4, v251\n"
    "s_and_b32 s4, s58, 1\n"
    "s_and_b32 s5, s57, 3\n"
    "s_lshl_b32 s36, s15, 3\n"
    "s_lshl_b32 s7, s5, 1\n"
    "s_add_u32 s36, s36, s7\n"
    "s_add_u32 s36, s36, s4\n"
    "s_lshl_b32 s4, s58, 6\n"
    "s_xor_b32 s5, s36, 0\n"
    "s_mul_i32 s5, s5, 0xc000\n"
    "s_add_u32 s40, s5, s4\n"
    "s_xor_b32 s5, s36, 1\n"
    "s_mul_i32 s5, s5, 0xc000\n"
    "s_add_u32 s41, s5, s4\n"
    "s_xor_b32 s5, s36, 2\n"
    "s_mul_i32 s5, s5, 0xc000\n"
    "s_add_u32 s42, s5, s4\n"
    "s_xor_b32 s5, s36, 3\n"
    "s_mul_i32 s5, s5, 0xc000\n"
    "s_add_u32 s43, s5, s4\n"
    "s_xor_b32 s5, s36, 4\n"
    "s_mul_i32 s5, s5, 0xc000\n"
    "s_add_u32 s48, s5, s4\n"
    "s_xor_b32 s5, s36, 5\n"
    "s_mul_i32 s5, s5, 0xc000\n"
    "s_add_u32 s49, s5, s4\n"
    "s_xor_b32 s5, s36, 6\n"
    "s_mul_i32 s5, s5, 0xc000\n"
    "s_add_u32 s50, s5, s4\n"
    "s_xor_b32 s5, s36, 7\n"
    "s_mul_i32 s5, s5, 0xc000\n"
    "s_add_u32 s51, s5, s4\n"
    "s_lshl_b32 s36, s36, 10\n"
    "buffer_load_dword v112, v252, s[8:11], s91 offen\n"
    "buffer_load_dword v113, v252, s[8:11], s91 offen offset:1024\n"
    "v_lshrrev_b32 v48, 4, v1\n"
    "v_and_b32 v49, 15, v1\n"
    "v_lshlrev_b32 v22, 4, v49\n"
    "v_lshl_or_b32 v22, v48, 12, v22\n"
    "v_lshlrev_b32 v247, 4, v4\n"
    "s_lshl_b32 s97, s60, 9\n"
    "s_lshl_b32 s7, s3, 8\n"
    "s_add_u32 s97, s97, s7\n"
    "s_add_u32 s97, s97, 0x40000\n"
    "s_lshl_b32 s4, s59, 1\n"
    "s_add_u32 s4, s4, s2\n"
    "s_lshl_b32 s39, s4, 9\n"
    "s_add_u32 s39, s39, 0x38000\n"
    "s_lshl_b32 s5, s4, 4\n"
    "s_lshl_b32 s7, s60, 1\n"
    "s_add_u32 s5, s5, s7\n"
    "s_add_u32 s5, s5, s3\n"
    "buffer_load_dword v114, v252, s[8:11], s91 offen offset:2048\n"
    "buffer_load_dword v115, v252, s[8:11], s91 offen offset:3072\n"
    "s_lshl_b32 s80, s5, 11\n"
    "s_add_u32 s80, s80, 0x200000\n"
    "s_lshl_b32 s81, s5, 8\n"
    "s_add_u32 s81, s81, 0x1c0000\n"
    "s_lshl_b32 s4, s15, 9\n"
    "v_lshl_add_u32 v24, v49, 2, s4\n"
    "v_lshl_add_u32 v24, v5, 6, v24\n"
    "v_lshl_add_u32 v13, v5, 4, s4\n"
    "v_lshlrev_b32 v19, 9, v1\n"
    "s_lshl_b32 s61, s57, 2\n"
    "s_add_u32 s61, s61, s15\n"
    "s_lshl_b32 s62, s57, 13\n"
    "s_lshl_b32 s7, s58, 3\n"
    "s_add_u32 s62, s62, s7\n"
    "s_add_u32 s62, s62, 0x1000000\n"
    "s_lshl_b32 s64, s57, 13\n"
    "s_lshl_b32 s7, s58, 9\n"
    "s_add_u32 s64, s64, s7\n"
    "s_add_u32 s64, s64, 0x1000000\n"
    "buffer_load_dword v116, v252, s[8:11], s92 offen\n"
    "buffer_load_dword v117, v252, s[8:11], s92 offen offset:1024\n"
    "s_lshl_b32 s63, s60, 14\n"
    "s_lshl_b32 s7, s59, 3\n"
    "s_add_u32 s63, s63, s7\n"
    "s_add_u32 s63, s63, 0x1020000\n"
    "s_lshl_b32 s65, s60, 14\n"
    "s_lshl_b32 s7, s59, 9\n"
    "s_add_u32 s65, s65, s7\n"
    "s_add_u32 s65, s65, 0x1020000\n"
    "s_lshl_b32 s66, s61, 15\n"
    "s_add_u32 s66, s66, 0x200000\n"
    "s_waitcnt vmcnt(14)\n"
    "v_lshlrev_b32 v32, 16, v172\n"
    "v_lshlrev_b32 v33, 16, v173\n"
    "v_lshlrev_b32 v34, 16, v174\n"
    "v_lshlrev_b32 v35, 16, v175\n"
    "v_and_b32 v36, s75, v172\n"
    "v_and_b32 v37, s75, v173\n"
    "buffer_load_dword v118, v252, s[8:11], s92 offen offset:2048\n"
    "buffer_load_dword v119, v252, s[8:11], s92 offen offset:3072\n"
    "v_and_b32 v38, s75, v174\n"
    "v_and_b32 v39, s75, v175\n"
    "s_waitcnt vmcnt(15)\n"
    "v_lshlrev_b32 v40, 16, v176\n"
    "v_lshlrev_b32 v41, 16, v177\n"
    "v_lshlrev_b32 v42, 16, v178\n"
    "v_lshlrev_b32 v43, 16, v179\n"
    "v_and_b32 v44, s75, v176\n"
    "v_and_b32 v45, s75, v177\n"
    "v_and_b32 v46, s75, v178\n"
    "v_and_b32 v47, s75, v179\n"
    "s_lshl_b32 s4, s15, 8\n"
    "v_add_u32 v10, s4, v2\n"
    "s_mov_b64 exec, 0xffff\n"
    "ds_write_b128 v10, v[168:171] offset:3072\n"
    "s_mov_b64 exec, -1\n"
    "s_add_u32 s89, s36, 0x4000\n"
    "s_sub_u32 m0, s89, 0x400\n"
    "s_nop 0\n"
    "buffer_load_dwordx4 v251, s[32:35], s40 offen offset:1024 lds\n"
    "s_xor_b32 s89, s89, 0x400\n"
    "s_sub_u32 m0, s89, 0x400\n"
    "s_nop 0\n"
    "buffer_load_dwordx4 v251, s[32:35], s41 offen offset:1024 lds\n"
    "s_xor_b32 s89, s89, 0xc00\n"
    "s_sub_u32 m0, s89, 0x400\n"
    "s_nop 0\n"
    "buffer_load_dwordx4 v251, s[32:35], s42 offen offset:1024 lds\n"
    "s_xor_b32 s89, s89, 0x400\n"
    "s_sub_u32 m0, s89, 0x400\n"
    "s_nop 0\n"
    "buffer_load_dwordx4 v251, s[32:35], s43 offen offset:1024 lds\n"
    "s_sleep 4\n"
    "buffer_load_dwordx4 v[80:83], v22, s[28:31], s97 offen sc1\n"
    "s_add_u32 s4, s97, 0x4000\n"
    "buffer_load_dwordx4 v[84:87], v22, s[28:31], s4 offen sc1\n"
    "buffer_load_dwordx4 v[88:91], v247, s[28:31], s39 offen sc1\n"
    "s_xor_b32 s89, s89, 0x1c00\n"
    "s_sub_u32 m0, s89, 0x400\n"
    "s_nop 0\n"
    "buffer_load_dwordx4 v251, s[32:35], s48 offen offset:1024 lds\n"
    "s_xor_b32 s89, s89, 0x400\n"
    "s_sub_u32 m0, s89, 0x400\n"
    "s_nop 0\n"
    "buffer_load_dwordx4 v251, s[32:35], s49 offen offset:1024 lds\n"
    "s_xor_b32 s89, s89, 0xc00\n"
    "s_sub_u32 m0, s89, 0x400\n"
    "s_nop 0\n"
    "buffer_load_dwordx4 v251, s[32:35], s50 offen offset:1024 lds\n"
    "s_xor_b32 s89, s89, 0x400\n"
    "s_sub_u32 m0, s89, 0x400\n"
    "s_nop 0\n"
    "buffer_load_dwordx4 v251, s[32:35], s51 offen offset:1024 lds\n"
    "s_waitcnt vmcnt(6)\n"
    "v_xor_b32 v10, s12, v81\n"
    "v_bitop3_b32 v10, v10, v83, s12 bitop3:0xf6\n"
    "s_waitcnt vmcnt(5)\n"
    "v_bitop3_b32 v10, v10, v85, s12 bitop3:0xf6\n"
    "v_bitop3_b32 v10, v10, v87, s12 bitop3:0xf6\n"
    "s_waitcnt vmcnt(4)\n"
    "v_bitop3_b32 v10, v10, v89, s12 bitop3:0xf6\n"
    "v_bitop3_b32 v10, v10, v91, s12 bitop3:0xf6\n"
    "v_cmp_ne_u32 vcc, 0, v10\n"
    "s_cbranch_vccz .Lq1\n"
    ".Lp1:\n"
    "buffer_load_dwordx4 v[80:83], v22, s[28:31], s97 offen sc1\n"
    "s_add_u32 s4, s97, 0x4000\n"
    "buffer_load_dwordx4 v[84:87], v22, s[28:31], s4 offen sc1\n"
    "buffer_load_dwordx4 v[88:91], v247, s[28:31], s39 offen sc1\n"
    "s_sub_i32 s14, s14, 1\n"
    "s_cmp_lt_i32 s14, 0\n"
    "s_waitcnt vmcnt(0)\n"
    "s_cbranch_scc1 .Lq1\n"
    "v_xor_b32 v10, s12, v81\n"
    "v_bitop3_b32 v10, v10, v83, s12 bitop3:0xf6\n"
    "v_bitop3_b32 v10, v10, v85, s12 bitop3:0xf6\n"
    "v_bitop3_b32 v10, v10, v87, s12 bitop3:0xf6\n"
    "v_bitop3_b32 v10, v10, v89, s12 bitop3:0xf6\n"
    "v_bitop3_b32 v10, v10, v91, s12 bitop3:0xf6\n"
    "v_cmp_ne_u32 vcc, 0, v10\n"
    "s_cbranch_vccnz .Lp1\n"
    ".Lq1:\n"
    "v_permlane32_swap_b32 v80, v82\n"
    "v_permlane32_swap_b32 v84, v86\n"
    "v_add_f32 v80, v80, v82\n"
    "v_add_f32 v84, v84, v86\n"
    "v_mov_b32 v82, v80\n"
    "v_mov_b32 v86, v84\n"
    "v_mul_f32 v88, 0x3b000000, v88\n"
    "v_mul_f32 v90, 0x3b000000, v90\n"
    "v_permlane16_swap_b32 v80, v82\n"
    "v_permlane16_swap_b32 v84, v86\n"
    "v_add_f32 v80, v80, v82\n"
    "v_add_f32 v84, v84, v86\n"
    "v_cvt_pk_bf16_f32 v80, v80, v80\n"
    "v_cvt_pk_bf16_f32 v84, v84, v84\n"
    "v_lshlrev_b32 v80, 16, v80\n"
    "v_lshlrev_b32 v84, 16, v84\n"
    "v_add_f32 v84, 1.0, v84\n"
    "v_cvt_pk_bf16_f32 v84, v84, v84\n"
    "v_lshlrev_b32 v84, 16, v84\n"
    "ds_write_b32 v24, v80 offset:10240\n"
    "ds_write_b32 v24, v84 offset:10368\n"
    "ds_read_b128 v[144:147], v13 offset:10240\n"
    "ds_read_b128 v[148:151], v13 offset:10304\n"
    "ds_read_b128 v[152:155], v13 offset:10272\n"
    "ds_read_b128 v[156:159], v13 offset:10336\n"
    "ds_read_b128 v[128:131], v13 offset:10368\n"
    "ds_read_b128 v[132:135], v13 offset:10432\n"
    "ds_read_b128 v[136:139], v13 offset:10400\n"
    "ds_read_b128 v[140:143], v13 offset:10464\n"
    "v_mul_f32 v89, v88, v88\n"
    "v_sub_f32 v90, v90, v89\n"
    "v_max_f32 v90, 0, v90\n"
    "v_add_f32 v90, 0x3727c5ac, v90\n"
    "v_rsq_f32 v90, v90\n"
    "s_nop 0\n"
    "v_mul_f32 v91, v88, v90\n"
    "v_pk_fma_f32 v[64:65], v[32:33], v[90:91], v[90:91] op_sel:[0,0,1] op_sel_hi:[1,0,1] neg_lo:[0,0,1] neg_hi:[0,0,1]\n"
    "v_pk_fma_f32 v[66:67], v[34:35], v[90:91], v[90:91] op_sel:[0,0,1] op_sel_hi:[1,0,1] neg_lo:[0,0,1] neg_hi:[0,0,1]\n"
    "v_pk_fma_f32 v[68:69], v[36:37], v[90:91], v[90:91] op_sel:[0,0,1] op_sel_hi:[1,0,1] neg_lo:[0,0,1] neg_hi:[0,0,1]\n"
    "v_pk_fma_f32 v[70:71], v[38:39], v[90:91], v[90:91] op_sel:[0,0,1] op_sel_hi:[1,0,1] neg_lo:[0,0,1] neg_hi:[0,0,1]\n"
    "v_pk_fma_f32 v[72:73], v[40:41], v[90:91], v[90:91] op_sel:[0,0,1] op_sel_hi:[1,0,1] neg_lo:[0,0,1] neg_hi:[0,0,1]\n"
    "v_pk_fma_f32 v[74:75], v[42:43], v[90:91], v[90:91] op_sel:[0,0,1] op_sel_hi:[1,0,1] neg_lo:[0,0,1] neg_hi:[0,0,1]\n"
    "v_pk_fma_f32 v[76:77], v[44:45], v[90:91], v[90:91] op_sel:[0,0,1] op_sel_hi:[1,0,1] neg_lo:[0,0,1] neg_hi:[0,0,1]\n"
    "v_pk_fma_f32 v[78:79], v[46:47], v[90:91], v[90:91] op_sel:[0,0,1] op_sel_hi:[1,0,1] neg_lo:[0,0,1] neg_hi:[0,0,1]\n"
    "s_waitcnt lgkmcnt(3)\n"
    "v_pk_fma_f32 v[64:65], v[64:65], v[128:129], v[144:145]\n"
    "v_pk_fma_f32 v[66:67], v[66:67], v[130:131], v[146:147]\n"
    "s_waitcnt lgkmcnt(2)\n"
    "v_pk_fma_f32 v[68:69], v[68:69], v[132:133], v[148:149]\n"
    "v_pk_fma_f32 v[70:71], v[70:71], v[134:135], v[150:151]\n"
    "v_cvt_pk_bf16_f32 v96, v64, v68\n"
    "v_cvt_pk_bf16_f32 v97, v65, v69\n"
    "v_cvt_pk_bf16_f32 v98, v66, v70\n"
    "v_cvt_pk_bf16_f32 v99, v67, v71\n"
    "buffer_store_dwordx4 v[96:99], v2, s[28:31], s80 offen sc1\n"
    "s_waitcnt lgkmcnt(1)\n"
    "v_pk_fma_f32 v[72:73], v[72:73], v[136:137], v[152:153]\n"
    "v_pk_fma_f32 v[74:75], v[74:75], v[138:139], v[154:155]\n"
    "s_waitcnt lgkmcnt(0)\n"
    "v_pk_fma_f32 v[76:77], v[76:77], v[140:141], v[156:157]\n"
    "v_pk_fma_f32 v[78:79], v[78:79], v[142:143], v[158:159]\n"
    "v_cvt_pk_bf16_f32 v100, v72, v76\n"
    "v_cvt_pk_bf16_f32 v101, v73, v77\n"
    "v_cvt_pk_bf16_f32 v102, v74, v78\n"
    "v_cvt_pk_bf16_f32 v103, v75, v79\n"
    "buffer_store_dwordx4 v[100:103], v2, s[28:31], s80 offen offset:1024 sc1\n"
    "v_bfe_u32 v128, v1, 2, 2\n"
    "v_bfe_u32 v129, v1, 4, 1\n"
    "v_lshrrev_b32 v10, 1, v27\n"
    "v_lshl_or_b32 v236, v129, 1, v10\n"
    "v_lshlrev_b32 v236, 3, v236\n"
    "v_and_b32 v131, 1, v1\n"
    "v_lshl_or_b32 v234, v131, 5, v236\n"
    "v_lshl_or_b32 v234, v128, 6, v234\n"
    "v_lshl_or_b32 v234, v5, 9, v234\n"
    "v_add_u32 v235, 0x1c000, v234\n"
    "v_add_u32 v234, 0x4000, v234\n"
    "v_lshl_or_b32 v236, v128, 5, v236\n"
    "v_lshl_or_b32 v236, v5, 9, v236\n"
    "s_lshl_b32 s4, s59, 2\n"
    "s_add_u32 s4, s4, s15\n"
    "s_bfe_u32 s5, s4, 0x20004\n"
    "s_and_b32 s6, s4, 15\n"
    "s_lshl_b32 s87, s5, 14\n"
    "s_lshl_b32 s7, s6, 8\n"
    "s_add_u32 s87, s87, s7\n"
    "s_add_u32 s87, s87, 0x48000\n"
    "s_cmp_eq_u32 s5, 2\n"
    "s_cselect_b32 s94, 1.0, 0\n"
    "s_and_b32 s5, s4, 63\n"
    "s_lshl_b32 s93, s60, 13\n"
    "s_lshl_b32 s7, s5, 7\n"
    "s_add_u32 s93, s93, s7\n"
    "s_add_u32 s93, s93, 0x60000\n"
    "v_perm_b32 v144, v184, v180, s72\n"
    "v_perm_b32 v145, v184, v180, s73\n"
    "v_dot2_f32_bf16 v160, v144, v110, 0\n"
    "v_dot2_f32_bf16 v161, v145, v110, 0\n"
    "s_add_u32 s89, s36, 0xc000\n"
    "s_sub_u32 m0, s89, 0x800\n"
    "v_perm_b32 v146, v185, v181, s72\n"
    "buffer_load_dwordx4 v251, s[32:35], s40 offen offset:2048 lds\n"
    "v_perm_b32 v147, v185, v181, s73\n"
    "v_dot2_f32_bf16 v162, v146, v110, 0\n"
    "v_dot2_f32_bf16 v163, v147, v110, 0\n"
    "v_perm_b32 v144, v186, v182, s72\n"
    "v_perm_b32 v145, v186, v182, s73\n"
    "v_dot2_f32_bf16 v164, v144, v110, 0\n"
    "v_dot2_f32_bf16 v165, v145, v110, 0\n"
    "v_perm_b32 v146, v187, v183, s72\n"
    "v_perm_b32 v147, v187, v183, s73\n"
    "v_dot2_f32_bf16 v166, v146, v110, 0\n"
    "v_dot2_f32_bf16 v167, v147, v110, 0\n"
    "v_perm_b32 v144, v192, v188, s72\n"
    "v_perm_b32 v145, v192, v188, s73\n"
    "v_dot2_f32_bf16 v160, v144, v111, v160\n"
    "v_dot2_f32_bf16 v161, v145, v111, v161\n"
    "v_perm_b32 v146, v193, v189, s72\n"
    "v_perm_b32 v147, v193, v189, s73\n"
    "v_dot2_f32_bf16 v162, v146, v111, v162\n"
    "v_dot2_f32_bf16 v163, v147, v111, v163\n"
    "v_perm_b32 v144, v194, v190, s72\n"
    "v_perm_b32 v145, v194, v190, s73\n"
    "v_dot2_f32_bf16 v164, v144, v111, v164\n"
    "v_dot2_f32_bf16 v165, v145, v111, v165\n"
    "v_perm_b32 v146, v195, v191, s72\n"
    "v_perm_b32 v147, v195, v191, s73\n"
    "v_dot2_f32_bf16 v166, v146, v111, v166\n"
    "v_dot2_f32_bf16 v167, v147, v111, v167\n"
    "v_permlane32_swap_b32 v160, v164\n"
    "v_permlane32_swap_b32 v161, v165\n"
    "v_permlane32_swap_b32 v162, v166\n"
    "v_permlane32_swap_b32 v163, v167\n"
    "v_add_f32 v160, v160, v164\n"
    "s_xor_b32 s89, s89, 0x400\n"
    "s_sub_u32 m0, s89, 0x800\n"
    "v_add_f32 v161, v161, v165\n"
    "buffer_load_dwordx4 v251, s[32:35], s41 offen offset:2048 lds\n"
    "v_add_f32 v162, v162, v166\n"
    "v_add_f32 v163, v163, v167\n"
    "s_nop 0\n"
    "v_permlane16_swap_b32 v160, v162\n"
    "v_permlane16_swap_b32 v161, v163\n"
    "v_add_f32 v160, v160, v162\n"
    "v_add_f32 v161, v161, v163\n"
    "s_nop 0\n"
    "v_add_f32_dpp v160, v160, v160 row_ror:8 row_mask:0xf bank_mask:0xf\n"
    "v_add_f32_dpp v161, v161, v161 row_ror:8 row_mask:0xf bank_mask:0xf\n"
    "s_mov_b32 vcc_lo, 0xff00ff00\n"
    "s_mov_b32 vcc_hi, 0xff00ff00\n"
    "v_cndmask_b32 v160, v160, v161, vcc\n"
    "ds_write_b32 v254, v160 offset:2048\n"
    "s_waitcnt lgkmcnt(0)\n"
    "s_waitcnt vmcnt(2)\n"
    "s_barrier\n"
    "s_cmp_lg_u32 s15, 1\n"
    "s_cbranch_scc1 .Ls2\n"
    "s_mov_b64 exec, 0xffff\n"
    "buffer_store_dwordx2 v[6:7], v19, s[28:31], s62 offen sc1\n"
    "s_mov_b64 exec, -1\n"
    ".Ls2:\n"
    "ds_read_b128 v[152:155], v2 offset:2048\n"
    "s_lshl_b32 s4, s4, 25\n"
    "s_and_b32 s4, s4, 0x80000000\n"
    "v_lshl_or_b32 v23, v1, 2, s4\n"
    "v_mov_b32 v132, 0x3f803f80\n"
    "v_accvgpr_write_b32 a160, v132\n"
    "v_accvgpr_write_b32 a161, v132\n"
    "v_accvgpr_write_b32 a162, v132\n"
    "v_accvgpr_write_b32 a163, v132\n"
    "v_mul_u32_u24 v249, 0xac0, v28\n"
    "v_lshl_add_u32 v249, v27, 4, v249\n"
    "s_cmp_lt_u32 s58, 11\n"
    "s_cselect_b32 s6, 0, 0x80000000\n"
    "v_or_b32 v250, s6, v249\n"
    "s_waitcnt lgkmcnt(0)\n"
    "v_pk_add_f32 v[152:153], v[152:153], v[154:155]\n"
    "v_add_f32 v152, v152, v153\n"
    "v_mov_b32 v153, s12\n"
    "buffer_store_dwordx2 v[152:153], v253, s[28:31], s90 offen sc1\n"
    "s_xor_b32 s89, s89, 0xc00\n"
    "s_sub_u32 m0, s89, 0x800\n"
    "v_or_b32 v20, s6, v2\n"
    "buffer_load_dwordx4 v251, s[32:35], s42 offen offset:2048 lds\n"
    "s_min_u32 s6, s58, 11\n"
    "s_lshl_b32 s38, s58, 1\n"
    "s_add_u32 s38, s38, s6\n"
    "s_lshl_b32 s4, s15, 11\n"
    "s_add_u32 s4, s4, 0x2000\n"
    "v_add_u32 v239, s4, v2\n"
    "s_lshl_b32 s4, s15, 12\n"
    "s_add_u32 s4, s4, 0x24000\n"
    "v_bfe_u32 v132, v1, 1, 2\n"
    "v_lshlrev_b32 v237, 4, v132\n"
    "v_lshl_or_b32 v237, v1, 6, v237\n"
    "v_add_u32 v237, s4, v237\n"
    "s_lshl_b32 s5, s3, 1\n"
    "v_lshrrev_b32 v238, 1, v27\n"
    "v_or_b32 v238, s5, v238\n"
    "v_lshrrev_b32 v132, 1, v128\n"
    "v_lshl_or_b32 v132, v129, 1, v132\n"
    "v_xor_b32 v238, v238, v132\n"
    "v_lshlrev_b32 v238, 4, v238\n"
    "v_lshl_or_b32 v238, v131, 3, v238\n"
    "v_lshl_or_b32 v238, v128, 6, v238\n"
    "v_lshl_or_b32 v238, v129, 8, v238\n"
    "v_lshl_or_b32 v238, v5, 10, v238\n"
    "s_lshl_b32 s5, s2, 11\n"
    "s_add_u32 s5, s5, 0x24000\n"
    "v_add_u32 v238, s5, v238\n"
    "s_lshl_b32 s5, s2, 6\n"
    "s_add_u32 s5, s5, 0x2000\n"
    "v_lshl_add_u32 v240, v27, 4, s5\n"
    "v_lshl_add_u32 v240, v128, 7, v240\n"
    "v_lshl_add_u32 v240, v129, 9, v240\n"
    "v_lshl_add_u32 v240, v5, 10, v240\n"
    "s_xor_b32 s89, s89, 0x400\n"
    "s_sub_u32 m0, s89, 0x800\n"
    "v_and_b32 v241, 48, v1\n"
    "buffer_load_dwordx4 v251, s[32:35], s43 offen offset:2048 lds\n"
    "v_lshl_or_b32 v241, v131, 6, v241\n"
    "s_mul_i32 s69, s58, 0xa000\n"
    "s_mul_i32 s7, s15, 0x280\n"
    "s_add_u32 s69, s69, s7\n"
    "s_add_u32 s69, s69, 0x80000\n"
    "s_mul_i32 s68, s57, 0xa00\n"
    "s_add_u32 s68, s68, s69\n"
    "v_mul_u32_u24 v243, 0xa00, v5\n"
    "v_add_u32 v243, v243, v247\n"
    "v_mul_u32_u24 v244, 0xa00, v28\n"
    "v_lshl_add_u32 v244, v27, 4, v244\n"
    "v_lshlrev_b32 v245, 5, v131\n"
    "v_lshl_or_b32 v245, v5, 4, v245\n"
    "v_bfe_u32 v132, v1, 1, 2\n"
    "v_lshl_or_b32 v245, v132, 7, v245\n"
    "v_bfe_u32 v132, v1, 3, 1\n"
    "v_lshl_or_b32 v245, v132, 3, v245\n"
    "v_lshl_or_b32 v245, v129, 9, v245\n"
    "s_lshl_b32 s5, s3, 10\n"
    "s_lshl_b32 s7, s2, 6\n"
    "s_add_u32 s5, s5, s7\n"
    "v_add_u32 v245, s5, v245\n"
    "v_lshrrev_b32 v130, 4, v1\n"
    "v_lshlrev_b32 v246, 1, v130\n"
    "v_lshl_or_b32 v246, v27, 3, v246\n"
    "s_lshl_b32 s5, s15, 5\n"
    "v_add_u32 v246, s5, v246\n"
    "v_lshlrev_b32 v242, 7, v28\n"
    "v_lshl_or_b32 v242, v27, 4, v242\n"
    "s_lshl_b32 s4, s60, 7\n"
    "s_lshl_b32 s7, s3, 6\n"
    "s_add_u32 s4, s4, s7\n"
    "s_add_u32 s4, s4, 0x2000\n"
    "s_xor_b32 s89, s89, 0x1c00\n"
    "s_sub_u32 m0, s89, 0x800\n"
    "v_lshl_add_u32 v13, v5, 4, s4\n"
    "buffer_load_dwordx4 v251, s[32:35], s48 offen offset:2048 lds\n"
    "v_lshlrev_b32 v248, 6, v5\n"
    "s_lshl_b32 s4, s3, 8\n"
    "v_xor_b32 v12, s4, v2\n"
    "s_lshl_b32 s71, s60, 18\n"
    "s_lshl_b32 s7, s15, 14\n"
    "s_add_u32 s71, s71, s7\n"
    "s_add_u32 s71, s71, 0xe00000\n"
    "s_lshl_b32 s76, s60, 18\n"
    "s_lshl_b32 s7, s15, 15\n"
    "s_add_u32 s76, s76, s7\n"
    "s_add_u32 s76, s76, 0xe10000\n"
    "s_lshl_b32 s70, s61, 15\n"
    "s_lshl_b32 s7, s58, 11\n"
    "s_add_u32 s70, s70, s7\n"
    "s_add_u32 s70, s70, 0x400000\n"
    "s_xor_b32 s89, s89, 0x400\n"
    "s_sub_u32 m0, s89, 0x800\n"
    "s_nop 0\n"
    "buffer_load_dwordx4 v251, s[32:35], s49 offen offset:2048 lds\n"
    "s_xor_b32 s89, s89, 0xc00\n"
    "s_sub_u32 m0, s89, 0x800\n"
    "s_nop 0\n"
    "buffer_load_dwordx4 v251, s[32:35], s50 offen offset:2048 lds\n"
    "s_xor_b32 s89, s89, 0x400\n"
    "s_sub_u32 m0, s89, 0x800\n"
    "s_nop 0\n"
    "buffer_load_dwordx4 v251, s[32:35], s51 offen offset:2048 lds\n"
    "s_add_u32 s62, s62, 0x40000\n"
    "s_sleep 5\n"
    "s_cmp_lg_u32 s15, 0\n"
    "s_cbranch_scc1 .Lr3\n"
    "v_not_b32 v31, v7\n"
    "s_mov_b64 exec, 0xffff\n"
    "buffer_load_dwordx2 v[30:31], v3, s[28:31], s64 offen sc1\n"
    "s_mov_b64 exec, -1\n"
    ".Lr3:\n"
    "s_add_u32 m0, s36, 0x1c000\n"
    "s_nop 0\n"
    "buffer_load_dwordx4 v251, s[32:35], s40 offen lds\n"
    "s_xor_b32 m0, m0, 0x400\n"
    "s_nop 0\n"
    "buffer_load_dwordx4 v251, s[32:35], s41 offen lds\n"
    "s_waitcnt vmcnt(2) lgkmcnt(0)\n"
    "s_cmp_lg_u32 s15, 0\n"
    "s_cbranch_scc1 .Lr4\n"
    "s_mov_b64 exec, 0xffff\n"
    "s_waitcnt vmcnt(2)\n"
    "v_cmp_ne_u64 vcc, s[12:13], v[30:31]\n"
    "s_cbranch_vccz .Lq4\n"
    ".Lp4:\n"
    "buffer_load_dwordx2 v[30:31], v3, s[28:31], s64 offen sc1\n"
    "s_sub_i32 s14, s14, 1\n"
    "s_cmp_lt_i32 s14, 0\n"
    "s_waitcnt vmcnt(0)\n"
    "s_cbranch_scc1 .Lq4\n"
    "v_cmp_ne_u64 vcc, s[12:13], v[30:31]\n"
    "s_cbranch_vccnz .Lp4\n"
    ".Lq4:\n"
    "s_mov_b64 exec, -1\n"
    ".Lr4:\n"
    "s_barrier\n"
    "s_add_u32 s64, s64, 0x40000\n"
    "buffer_load_dwordx4 a[0:3], v2, s[28:31], s66 offen sc1\n"
    "ds_read_b64_tr_b16 v[196:197], v236 offset:3072\n"
    "ds_read_b64_tr_b16 v[198:199], v236 offset:3200\n"
    "ds_read_b64_tr_b16 v[200:201], v236 offset:3328\n"
    "ds_read_b64_tr_b16 v[202:203], v236 offset:3456\n"
    "ds_read_b64_tr_b16 a[128:129], v234\n"
    "ds_read_b64_tr_b16 a[130:131], v234 offset:256\n"
    "ds_read_b64_tr_b16 a[132:133], v234 offset:32768\n"
    "ds_read_b64_tr_b16 a[134:135], v234 offset:33024\n"
    "ds_read_b64_tr_b16 a[136:137], v234 offset:1024\n"
    "ds_read_b64_tr_b16 a[138:139], v234 offset:1280\n"
    "ds_read_b64_tr_b16 a[140:141], v234 offset:33792\n"
    "buffer_load_dwordx4 a[4:7], v2, s[28:31], s66 offen offset:1024 sc1\n"
    "ds_read_b64_tr_b16 a[142:143], v234 offset:34048\n"
    "s_mov_b32 vcc_lo, 0xf0f0f0f\n"
    "s_mov_b32 vcc_hi, 0xf0f0f0f0\n"
    "s_waitcnt lgkmcnt(11)\n"
    "v_cndmask_b32 v196, 0, v196, vcc\n"
    "v_cndmask_b32 v197, 0, v197, vcc\n"
    "s_waitcnt lgkmcnt(10)\n"
    "v_cndmask_b32 v198, 0, v198, vcc\n"
    "v_cndmask_b32 v199, 0, v199, vcc\n"
    "s_waitcnt lgkmcnt(9)\n"
    "v_cndmask_b32 v200, 0, v200, vcc\n"
    "v_cndmask_b32 v201, 0, v201, vcc\n"
    "s_waitcnt lgkmcnt(8)\n"
    "v_cndmask_b32 v202, 0, v202, vcc\n"
    "v_cndmask_b32 v203, 0, v203, vcc\n"
    "buffer_load_dwordx4 a[8:11], v2, s[28:31], s66 offen offset:2048 sc1\n"
    "v_accvgpr_write_b32 a152, v196\n"
    "v_accvgpr_write_b32 a153, v197\n"
    "v_accvgpr_write_b32 a154, v198\n"
    "v_accvgpr_write_b32 a155, v199\n"
    "v_accvgpr_write_b32 a156, v200\n"
    "v_accvgpr_write_b32 a157, v201\n"
    "v_accvgpr_write_b32 a158, v202\n"
    "v_accvgpr_write_b32 a159, v203\n"
    "s_nop 2\n"
    "buffer_load_dwordx4 a[12:15], v2, s[28:31], s66 offen offset:3072 sc1\n"
    "s_nop 10\n"
    "s_add_u32 s66, s66, 0x1000\n"
    "buffer_load_dwordx4 a[16:19], v2, s[28:31], s66 offen sc1\n"
    "s_nop 9\n"
    "buffer_load_dwordx4 a[20:23], v2, s[28:31], s66 offen offset:1024 sc1\n"
    "s_nop 10\n"
    "buffer_load_dwordx4 a[24:27], v2, s[28:31], s66 offen offset:2048 sc1\n"
    "s_nop 10\n"
    "buffer_load_dwordx4 a[28:31], v2, s[28:31], s66 offen offset:3072 sc1\n"
    "s_waitcnt vmcnt(7) lgkmcnt(6)\n"
    "v_mfma_f32_32x32x16_bf16 v[64:79], a[128:131], a[0:3], 0\n"
    "ds_read_b64_tr_b16 a[144:145], v234 offset:2048\n"
    "ds_read_b64_tr_b16 a[146:147], v234 offset:2304\n"
    "s_add_u32 s66, s66, 0x1000\n"
    "buffer_load_dwordx4 a[32:35], v2, s[28:31], s66 offen sc1\n"
    "s_waitcnt lgkmcnt(6)\n"
    "v_mfma_f32_32x32x16_bf16 v[80:95], a[0:3], a[132:135], 0\n"
    "ds_read_b64_tr_b16 a[148:149], v234 offset:34816\n"
    "ds_read_b64_tr_b16 a[150:151], v234 offset:35072\n"
    "v_perm_b32 v120, v113, v112, s72\n"
    "v_perm_b32 v121, v115, v114, s72\n"
    "s_waitcnt vmcnt(7) lgkmcnt(6)\n"
    "v_mfma_f32_32x32x16_bf16 v[64:79], a[136:139], a[4:7], v[64:79]\n"
    "ds_read_b64_tr_b16 a[128:129], v234 offset:3072\n"
    "ds_read_b64_tr_b16 a[130:131], v234 offset:3328\n"
    "buffer_load_dwordx4 a[36:39], v2, s[28:31], s66 offen offset:1024 sc1\n"
    "s_waitcnt lgkmcnt(6)\n"
    "v_mfma_f32_32x32x16_bf16 v[80:95], a[4:7], a[140:143], v[80:95]\n"
    "ds_read_b64_tr_b16 a[132:133], v234 offset:35840\n"
    "ds_read_b64_tr_b16 a[134:135], v234 offset:36096\n"
    "v_perm_b32 v122, v117, v116, s72\n"
    "v_perm_b32 v123, v119, v118, s72\n"
    "s_waitcnt vmcnt(7) lgkmcnt(6)\n"
    "v_mfma_f32_32x32x16_bf16 v[64:79], a[144:147], a[8:11], v[64:79]\n"
    "ds_read_b64_tr_b16 a[136:137], v234 offset:4096\n"
    "ds_read_b64_tr_b16 a[138:139], v234 offset:4352\n"
    "buffer_load_dwordx4 a[40:43], v2, s[28:31], s66 offen offset:2048 sc1\n"
    "s_waitcnt lgkmcnt(6)\n"
    "v_mfma_f32_32x32x16_bf16 v[80:95], a[8:11], a[148:151], v[80:95]\n"
    "ds_read_b64_tr_b16 a[140:141], v234 offset:36864\n"
    "ds_read_b64_tr_b16 a[142:143], v234 offset:37120\n"
    "v_perm_b32 v124, v113, v112, s73\n"
    "v_perm_b32 v125, v115, v114, s73\n"
    "s_waitcnt vmcnt(7) lgkmcnt(6)\n"
    "v_mfma_f32_32x32x16_bf16 v[64:79], a[128:131], a[12:15], v[64:79]\n"
    "ds_read_b64_tr_b16 a[144:145], v234 offset:5120\n"
    "ds_read_b64_tr_b16 a[146:147], v234 offset:5376\n"
    "buffer_load_dwordx4 a[44:47], v2, s[28:31], s66 offen offset:3072 sc1\n"
    "s_waitcnt lgkmcnt(6)\n"
    "v_mfma_f32_32x32x16_bf16 v[80:95], a[12:15], a[132:135], v[80:95]\n"
    "ds_read_b64_tr_b16 a[148:149], v234 offset:37888\n"
    "ds_read_b64_tr_b16 a[150:151], v234 offset:38144\n"
    "v_perm_b32 v126, v117, v116, s73\n"
    "v_perm_b32 v127, v119, v118, s73\n"
    "s_waitcnt vmcnt(7) lgkmcnt(6)\n"
    "v_mfma_f32_32x32x16_bf16 v[64:79], a[136:139], a[16:19], v[64:79]\n"
    "ds_read_b64_tr_b16 a[128:129], v234 offset:6144\n"
    "ds_read_b64_tr_b16 a[130:131], v234 offset:6400\n"
    "s_add_u32 s66, s66, 0x1000\n"
    "buffer_load_dwordx4 a[48:51], v2, s[28:31], s66 offen sc1\n"
    "s_waitcnt lgkmcnt(6)\n"
    "v_mfma_f32_32x32x16_bf16 v[80:95], a[16:19], a[140:143], v[80:95]\n"
    "ds_read_b64_tr_b16 a[132:133], v234 offset:38912\n"
    "ds_read_b64_tr_b16 a[134:135], v234 offset:39168\n"
    "v_xor_b32 v133, 16, v237\n"
    "v_xor_b32 v134, 32, v237\n"
    "s_waitcnt vmcnt(7) lgkmcnt(6)\n"
    "v_mfma_f32_32x32x16_bf16 v[64:79], a[144:147], a[20:23], v[64:79]\n"
    "ds_read_b64_tr_b16 a[136:137], v234 offset:7168\n"
    "ds_read_b64_tr_b16 a[138:139], v234 offset:7424\n"
    "buffer_load_dwordx4 a[52:55], v2, s[28:31], s66 offen offset:1024 sc1\n"
    "s_waitcnt lgkmcnt(6)\n"
    "v_mfma_f32_32x32x16_bf16 v[80:95], a[20:23], a[148:151], v[80:95]\n"
    "ds_read_b64_tr_b16 a[140:141], v234 offset:39936\n"
    "ds_read_b64_tr_b16 a[142:143], v234 offset:40192\n"
    "v_xor_b32 v135, 48, v237\n"
    "s_waitcnt vmcnt(7) lgkmcnt(6)\n"
    "v_mfma_f32_32x32x16_bf16 v[64:79], a[128:131], a[24:27], v[64:79]\n"
    "ds_read_b64_tr_b16 a[144:145], v234 offset:8192\n"
    "ds_read_b64_tr_b16 a[146:147], v234 offset:8448\n"
    "buffer_load_dwordx4 a[56:59], v2, s[28:31], s66 offen offset:2048 sc1\n"
    "s_waitcnt lgkmcnt(6)\n"
    "v_mfma_f32_32x32x16_bf16 v[80:95], a[24:27], a[132:135], v[80:95]\n"
    "ds_read_b64_tr_b16 a[148:149], v234 offset:40960\n"
    "ds_read_b64_tr_b16 a[150:151], v234 offset:41216\n"
    "s_waitcnt vmcnt(7) lgkmcnt(6)\n"
    "v_mfma_f32_32x32x16_bf16 v[64:79], a[136:139], a[28:31], v[64:79]\n"
    "ds_read_b64_tr_b16 a[128:129], v234 offset:9216\n"
    "ds_read_b64_tr_b16 a[130:131], v234 offset:9472\n"
    "buffer_load_dwordx4 a[60:63], v2, s[28:31], s66 offen offset:3072 sc1\n"
    "s_waitcnt lgkmcnt(6)\n"
    "v_mfma_f32_32x32x16_bf16 v[80:95], a[28:31], a[140:143], v[80:95]\n"
    "ds_read_b64_tr_b16 a[132:133], v234 offset:41984\n"
    "ds_read_b64_tr_b16 a[134:135], v234 offset:42240\n"
    "s_waitcnt vmcnt(7) lgkmcnt(6)\n"
    "v_mfma_f32_32x32x16_bf16 v[64:79], a[144:147], a[32:35], v[64:79]\n"
    "ds_read_b64_tr_b16 a[136:137], v234 offset:10240\n"
    "ds_read_b64_tr_b16 a[138:139], v234 offset:10496\n"
    "s_add_u32 s66, s66, 0x1000\n"
    "buffer_load_dwordx4 a[64:67], v2, s[28:31], s66 offen sc1\n"
    "s_waitcnt lgkmcnt(6)\n"
    "v_mfma_f32_32x32x16_bf16 v[80:95], a[32:35], a[148:151], v[80:95]\n"
    "ds_read_b64_tr_b16 a[140:141], v234 offset:43008\n"
    "ds_read_b64_tr_b16 a[142:143], v234 offset:43264\n"
    "s_waitcnt vmcnt(7) lgkmcnt(6)\n"
    "v_mfma_f32_32x32x16_bf16 v[64:79], a[128:131], a[36:39], v[64:79]\n"
    "ds_read_b64_tr_b16 a[144:145], v234 offset:11264\n"
    "ds_read_b64_tr_b16 a[146:147], v234 offset:11520\n"
    "buffer_load_dwordx4 a[68:71], v2, s[28:31], s66 offen offset:1024 sc1\n"
    "s_waitcnt lgkmcnt(6)\n"
    "v_mfma_f32_32x32x16_bf16 v[80:95], a[36:39], a[132:135], v[80:95]\n"
    "ds_read_b64_tr_b16 a[148:149], v234 offset:44032\n"
    "ds_read_b64_tr_b16 a[150:151], v234 offset:44288\n"
    "s_waitcnt vmcnt(7) lgkmcnt(6)\n"
    "v_mfma_f32_32x32x16_bf16 v[64:79], a[136:139], a[40:43], v[64:79]\n"
    "ds_read_b64_tr_b16 a[128:129], v234 offset:12288\n"
    "ds_read_b64_tr_b16 a[130:131], v234 offset:12544\n"
    "buffer_load_dwordx4 a[72:75], v2, s[28:31], s66 offen offset:2048 sc1\n"
    "s_waitcnt lgkmcnt(6)\n"
    "v_mfma_f32_32x32x16_bf16 v[80:95], a[40:43], a[140:143], v[80:95]\n"
    "ds_read_b64_tr_b16 a[132:133], v234 offset:45056\n"
    "ds_read_b64_tr_b16 a[134:135], v234 offset:45312\n"
    "s_waitcnt vmcnt(7) lgkmcnt(6)\n"
    "v_mfma_f32_32x32x16_bf16 v[64:79], a[144:147], a[44:47], v[64:79]\n"
    "ds_read_b64_tr_b16 a[136:137], v234 offset:13312\n"
    "ds_read_b64_tr_b16 a[138:139], v234 offset:13568\n"
    "buffer_load_dwordx4 a[76:79], v2, s[28:31], s66 offen offset:3072 sc1\n"
    "s_waitcnt lgkmcnt(6)\n"
    "v_mfma_f32_32x32x16_bf16 v[80:95], a[44:47], a[148:151], v[80:95]\n"
    "ds_read_b64_tr_b16 a[140:141], v234 offset:46080\n"
    "ds_read_b64_tr_b16 a[142:143], v234 offset:46336\n"
    "s_waitcnt vmcnt(7) lgkmcnt(6)\n"
    "v_mfma_f32_32x32x16_bf16 v[64:79], a[128:131], a[48:51], v[64:79]\n"
    "ds_read_b64_tr_b16 a[144:145], v234 offset:14336\n"
    "ds_read_b64_tr_b16 a[146:147], v234 offset:14592\n"
    "buffer_load_dwordx4 v[104:107], v22, s[28:31], s87 offen sc1\n"
    "s_waitcnt lgkmcnt(6)\n"
    "v_mfma_f32_32x32x16_bf16 v[80:95], a[48:51], a[132:135], v[80:95]\n"
    "ds_read_b64_tr_b16 a[148:149], v234 offset:47104\n"
    "ds_read_b64_tr_b16 a[150:151], v234 offset:47360\n"
    "s_waitcnt vmcnt(7) lgkmcnt(6)\n"
    "v_mfma_f32_32x32x16_bf16 v[64:79], a[136:139], a[52:55], v[64:79]\n"
    "ds_read_b64_tr_b16 a[128:129], v234 offset:15360\n"
    "ds_read_b64_tr_b16 a[130:131], v234 offset:15616\n"
    "s_add_u32 s66, s66, 0x1000\n"
    "buffer_load_dwordx4 a[80:83], v2, s[28:31], s66 offen sc1\n"
    "s_waitcnt lgkmcnt(6)\n"
    "v_mfma_f32_32x32x16_bf16 v[80:95], a[52:55], a[140:143], v[80:95]\n"
    "ds_read_b64_tr_b16 a[132:133], v234 offset:48128\n"
    "ds_read_b64_tr_b16 a[134:135], v234 offset:48384\n"
    "s_waitcnt vmcnt(7) lgkmcnt(6)\n"
    "v_mfma_f32_32x32x16_bf16 v[64:79], a[144:147], a[56:59], v[64:79]\n"
    "ds_read_b64_tr_b16 a[136:137], v234 offset:16384\n"
    "ds_read_b64_tr_b16 a[138:139], v234 offset:16640\n"
    "buffer_load_dwordx4 a[84:87], v2, s[28:31], s66 offen offset:1024 sc1\n"
    "s_waitcnt lgkmcnt(6)\n"
    "v_mfma_f32_32x32x16_bf16 v[80:95], a[56:59], a[148:151], v[80:95]\n"
    "ds_read_b64_tr_b16 a[140:141], v234 offset:49152\n"
    "ds_read_b64_tr_b16 a[142:143], v234 offset:49408\n"
    "s_waitcnt vmcnt(7) lgkmcnt(6)\n"
    "v_mfma_f32_32x32x16_bf16 v[64:79], a[128:131], a[60:63], v[64:79]\n"
    "ds_read_b64_tr_b16 a[144:145], v234 offset:17408\n"
    "ds_read_b64_tr_b16 a[146:147], v234 offset:17664\n"
    "buffer_load_dwordx4 a[88:91], v2, s[28:31], s66 offen offset:2048 sc1\n"
    "s_waitcnt lgkmcnt(6)\n"
    "v_mfma_f32_32x32x16_bf16 v[80:95], a[60:63], a[132:135], v[80:95]\n"
    "ds_read_b64_tr_b16 a[148:149], v234 offset:50176\n"
    "ds_read_b64_tr_b16 a[150:151], v234 offset:50432\n"
    "s_waitcnt vmcnt(7) lgkmcnt(6)\n"
    "v_mfma_f32_32x32x16_bf16 v[64:79], a[136:139], a[64:67], v[64:79]\n"
    "ds_read_b64_tr_b16 a[128:129], v234 offset:18432\n"
    "ds_read_b64_tr_b16 a[130:131], v234 offset:18688\n"
    "buffer_load_dwordx4 a[92:95], v2, s[28:31], s66 offen offset:3072 sc1\n"
    "s_waitcnt lgkmcnt(6)\n"
    "v_mfma_f32_32x32x16_bf16 v[80:95], a[64:67], a[140:143], v[80:95]\n"
    "ds_read_b64_tr_b16 a[132:133], v234 offset:51200\n"
    "ds_read_b64_tr_b16 a[134:135], v234 offset:51456\n"
    "s_waitcnt vmcnt(7) lgkmcnt(6)\n"
    "v_mfma_f32_32x32x16_bf16 v[64:79], a[144:147], a[68:71], v[64:79]\n"
    "ds_read_b64_tr_b16 a[136:137], v234 offset:19456\n"
    "ds_read_b64_tr_b16 a[138:139], v234 offset:19712\n"
    "s_add_u32 s66, s66, 0x1000\n"
    "buffer_load_dwordx4 a[96:99], v2, s[28:31], s66 offen sc1\n"
    "s_waitcnt lgkmcnt(6)\n"
    "v_mfma_f32_32x32x16_bf16 v[80:95], a[68:71], a[148:151], v[80:95]\n"
    "ds_read_b64_tr_b16 a[140:141], v234 offset:52224\n"
    "ds_read_b64_tr_b16 a[142:143], v234 offset:52480\n"
    "s_waitcnt vmcnt(7) lgkmcnt(6)\n"
    "v_mfma_f32_32x32x16_bf16 v[64:79], a[128:131], a[72:75], v[64:79]\n"
    "ds_read_b64_tr_b16 a[144:145], v234 offset:20480\n"
    "ds_read_b64_tr_b16 a[146:147], v234 offset:20736\n"
    "buffer_load_dwordx4 a[100:103], v2, s[28:31], s66 offen offset:1024 sc1\n"
    "s_waitcnt lgkmcnt(6)\n"
    "v_mfma_f32_32x32x16_bf16 v[80:95], a[72:75], a[132:135], v[80:95]\n"
    "ds_read_b64_tr_b16 a[148:149], v234 offset:53248\n"
    "ds_read_b64_tr_b16 a[150:151], v234 offset:53504\n"
    "s_waitcnt vmcnt(7) lgkmcnt(6)\n"
    "v_mfma_f32_32x32x16_bf16 v[64:79], a[136:139], a[76:79], v[64:79]\n"
    "ds_read_b64_tr_b16 a[128:129], v234 offset:21504\n"
    "ds_read_b64_tr_b16 a[130:131], v234 offset:21760\n"
    "buffer_load_dwordx4 a[104:107], v2, s[28:31], s66 offen offset:2048 sc1\n"
    "s_waitcnt lgkmcnt(6)\n"
    "v_mfma_f32_32x32x16_bf16 v[80:95], a[76:79], a[140:143], v[80:95]\n"
    "ds_read_b64_tr_b16 a[132:133], v234 offset:54272\n"
    "ds_read_b64_tr_b16 a[134:135], v234 offset:54528\n"
    "s_waitcnt vmcnt(6) lgkmcnt(6)\n"
    "v_mfma_f32_32x32x16_bf16 v[64:79], a[144:147], a[80:83], v[64:79]\n"
    "ds_read_b64_tr_b16 a[136:137], v234 offset:22528\n"
    "ds_read_b64_tr_b16 a[138:139], v234 offset:22784\n"
    "buffer_load_dwordx4 a[108:111], v2, s[28:31], s66 offen offset:3072 sc1\n"
    "s_waitcnt lgkmcnt(6)\n"
    "v_mfma_f32_32x32x16_bf16 v[80:95], a[80:83], a[148:151], v[80:95]\n"
    "ds_read_b64_tr_b16 a[140:141], v234 offset:55296\n"
    "ds_read_b64_tr_b16 a[142:143], v234 offset:55552\n"
    "s_waitcnt vmcnt(6) lgkmcnt(6)\n"
    "v_mfma_f32_32x32x16_bf16 v[64:79], a[128:131], a[84:87], v[64:79]\n"
    "ds_read_b64_tr_b16 a[144:145], v234 offset:23552\n"
    "ds_read_b64_tr_b16 a[146:147], v234 offset:23808\n"
    "s_add_u32 s66, s66, 0x1000\n"
    "buffer_load_dwordx4 a[112:115], v2, s[28:31], s66 offen sc1\n"
    "s_waitcnt lgkmcnt(6)\n"
    "v_mfma_f32_32x32x16_bf16 v[80:95], a[84:87], a[132:135], v[80:95]\n"
    "ds_read_b64_tr_b16 a[148:149], v234 offset:56320\n"
    "ds_read_b64_tr_b16 a[150:151], v234 offset:56576\n"
    "v_xor_b32 v10, s12, v105\n"
    "v_bitop3_b32 v10, v10, v107, s12 bitop3:0xf6\n"
    "v_cmp_ne_u32 vcc, 0, v10\n"
    "s_cbranch_vccz .Lq5\n"
    ".Lp5:\n"
    "buffer_load_dwordx4 v[104:107], v22, s[28:31], s87 offen sc1\n"
    "s_sub_i32 s14, s14, 1\n"
    "s_cmp_lt_i32 s14, 0\n"
    "s_waitcnt vmcnt(0)\n"
    "s_cbranch_scc1 .Lq5\n"
    "v_xor_b32 v10, s12, v105\n"
    "v_bitop3_b32 v10, v10, v107, s12 bitop3:0xf6\n"
    "v_cmp_ne_u32 vcc, 0, v10\n"
    "s_cbranch_vccnz .Lp5\n"
    ".Lq5:\n"
    "v_permlane32_swap_b32 v104, v106\n"
    "s_waitcnt vmcnt(6) lgkmcnt(6)\n"
    "v_mfma_f32_32x32x16_bf16 v[64:79], a[136:139], a[88:91], v[64:79]\n"
    "ds_read_b64_tr_b16 a[128:129], v234 offset:24576\n"
    "ds_read_b64_tr_b16 a[130:131], v234 offset:24832\n"
    "buffer_load_dwordx4 a[116:119], v2, s[28:31], s66 offen offset:1024 sc1\n"
    "s_waitcnt lgkmcnt(6)\n"
    "v_mfma_f32_32x32x16_bf16 v[80:95], a[88:91], a[140:143], v[80:95]\n"
    "ds_read_b64_tr_b16 a[132:133], v234 offset:57344\n"
    "ds_read_b64_tr_b16 a[134:135], v234 offset:57600\n"
    "v_add_f32 v104, v104, v106\n"
    "v_mov_b32 v106, v104\n"
    "s_waitcnt vmcnt(6) lgkmcnt(6)\n"
    "v_mfma_f32_32x32x16_bf16 v[64:79], a[144:147], a[92:95], v[64:79]\n"
    "ds_read_b64_tr_b16 a[136:137], v234 offset:25600\n"
    "ds_read_b64_tr_b16 a[138:139], v234 offset:25856\n"
    "buffer_load_dwordx4 a[120:123], v2, s[28:31], s66 offen offset:2048 sc1\n"
    "s_waitcnt lgkmcnt(6)\n"
    "v_mfma_f32_32x32x16_bf16 v[80:95], a[92:95], a[148:151], v[80:95]\n"
    "ds_read_b64_tr_b16 a[140:141], v234 offset:58368\n"
    "ds_read_b64_tr_b16 a[142:143], v234 offset:58624\n"
    "v_permlane16_swap_b32 v104, v106\n"
    "s_waitcnt vmcnt(6) lgkmcnt(6)\n"
    "v_mfma_f32_32x32x16_bf16 v[64:79], a[128:131], a[96:99], v[64:79]\n"
    "ds_read_b64_tr_b16 a[144:145], v234 offset:26624\n"
    "ds_read_b64_tr_b16 a[146:147], v234 offset:26880\n"
    "buffer_load_dwordx4 a[124:127], v2, s[28:31], s66 offen offset:3072 sc1\n"
    "s_waitcnt lgkmcnt(6)\n"
    "v_mfma_f32_32x32x16_bf16 v[80:95], a[96:99], a[132:135], v[80:95]\n"
    "ds_read_b64_tr_b16 a[148:149], v234 offset:59392\n"
    "ds_read_b64_tr_b16 a[150:151], v234 offset:59648\n"
    "v_add_f32 v104, v104, v106\n"
    "v_cvt_pk_bf16_f32 v104, v104, v104\n"
    "s_waitcnt vmcnt(6) lgkmcnt(6)\n"
    "v_mfma_f32_32x32x16_bf16 v[64:79], a[136:139], a[100:103], v[64:79]\n"
    "ds_read_b64_tr_b16 a[128:129], v234 offset:27648\n"
    "ds_read_b64_tr_b16 a[130:131], v234 offset:27904\n"
    "buffer_store_dwordx4 v[120:123], v2, s[28:31], s67 offen sc1\n"
    "s_waitcnt lgkmcnt(6)\n"
    "v_mfma_f32_32x32x16_bf16 v[80:95], a[100:103], a[140:143], v[80:95]\n"
    "ds_read_b64_tr_b16 a[132:133], v234 offset:60416\n"
    "ds_read_b64_tr_b16 a[134:135], v234 offset:60672\n"
    "v_lshlrev_b32 v104, 16, v104\n"
    "v_add_f32 v104, s94, v104\n"
    "s_waitcnt vmcnt(6) lgkmcnt(6)\n"
    "v_mfma_f32_32x32x16_bf16 v[64:79], a[144:147], a[104:107], v[64:79]\n"
    "ds_read_b64_tr_b16 a[136:137], v234 offset:28672\n"
    "ds_read_b64_tr_b16 a[138:139], v234 offset:28928\n"
    "s_waitcnt lgkmcnt(6)\n"
    "v_mfma_f32_32x32x16_bf16 v[80:95], a[104:107], a[148:151], v[80:95]\n"
    "ds_read_b64_tr_b16 a[140:141], v234 offset:61440\n"
    "ds_read_b64_tr_b16 a[142:143], v234 offset:61696\n"
    "v_mov_b32 v106, v104\n"
    "s_waitcnt vmcnt(5) lgkmcnt(6)\n"
    "v_mfma_f32_32x32x16_bf16 v[64:79], a[128:131], a[108:111], v[64:79]\n"
    "ds_read_b64_tr_b16 a[144:145], v234 offset:29696\n"
    "ds_read_b64_tr_b16 a[146:147], v234 offset:29952\n"
    "s_waitcnt lgkmcnt(6)\n"
    "v_mfma_f32_32x32x16_bf16 v[80:95], a[108:111], a[132:135], v[80:95]\n"
    "ds_read_b64_tr_b16 a[148:149], v234 offset:62464\n"
    "ds_read_b64_tr_b16 a[150:151], v234 offset:62720\n"
    "v_permlane32_swap_b32 v104, v106\n"
    "v_cvt_pk_bf16_f32 v104, v104, v106\n"
    "s_waitcnt vmcnt(4) lgkmcnt(6)\n"
    "v_mfma_f32_32x32x16_bf16 v[64:79], a[136:139], a[112:115], v[64:79]\n"
    "ds_read_b64_tr_b16 a[128:129], v234 offset:30720\n"
    "ds_read_b64_tr_b16 a[130:131], v234 offset:30976\n"
    "buffer_store_dwordx4 v[124:127], v2, s[28:31], s67 offen offset:1024 sc1\n"
    "s_waitcnt lgkmcnt(6)\n"
    "v_mfma_f32_32x32x16_bf16 v[80:95], a[112:115], a[140:143], v[80:95]\n"
    "ds_read_b64_tr_b16 a[132:133], v234 offset:63488\n"
    "ds_read_b64_tr_b16 a[134:135], v234 offset:63744\n"
    "s_mov_b32 exec_hi, 0\n"
    "buffer_store_dword v104, v23, s[28:31], s93 offen sc1\n"
    "s_mov_b64 exec, -1\n"
    "s_waitcnt vmcnt(5) lgkmcnt(6)\n"
    "v_mfma_f32_32x32x16_bf16 v[64:79], a[144:147], a[116:119], v[64:79]\n"
    "ds_read_b64_tr_b16 a[136:137], v234 offset:31744\n"
    "ds_read_b64_tr_b16 a[138:139], v234 offset:32000\n"
    "s_waitcnt lgkmcnt(6)\n"
    "v_mfma_f32_32x32x16_bf16 v[80:95], a[116:119], a[148:151], v[80:95]\n"
    "ds_read_b64_tr_b16 a[140:141], v234 offset:64512\n"
    "ds_read_b64_tr_b16 a[142:143], v234 offset:64768\n"
    "s_waitcnt vmcnt(4) lgkmcnt(6)\n"
    "v_mfma_f32_32x32x16_bf16 v[64:79], a[128:131], a[120:123], v[64:79]\n"
    "s_waitcnt lgkmcnt(4)\n"
    "v_mfma_f32_32x32x16_bf16 v[80:95], a[120:123], a[132:135], v[80:95]\n"
    "s_waitcnt vmcnt(3) lgkmcnt(2)\n"
    "v_mfma_f32_32x32x16_bf16 v[64:79], a[136:139], a[124:127], v[64:79]\n"
    "s_xor_b32 m0, m0, 0xc00\n"
    "s_nop 0\n"
    "buffer_load_dwordx4 v251, s[32:35], s42 offen lds\n"
    "s_waitcnt lgkmcnt(0)\n"
    "v_mfma_f32_32x32x16_bf16 v[80:95], a[124:127], a[140:143], v[80:95]\n"
    "s_barrier\n"
    "s_nop 5\n"
    "v_cvt_pk_bf16_f32 v48, v64, v65\n"
    "v_cvt_pk_bf16_f32 v49, v66, v67\n"
    "v_cvt_pk_bf16_f32 v50, v68, v69\n"
    "v_cvt_pk_bf16_f32 v51, v70, v71\n"
    "s_nop 1\n"
    "v_mfma_f32_32x32x16_bf16 v[180:195], a[152:155], v[48:51], 0\n"
    "v_cvt_pk_bf16_f32 v52, v72, v73\n"
    "v_cvt_pk_bf16_f32 v53, v74, v75\n"
    "v_cvt_pk_bf16_f32 v54, v76, v77\n"
    "v_cvt_pk_bf16_f32 v55, v78, v79\n"
    "s_nop 1\n"
    "v_mfma_f32_32x32x16_bf16 v[180:195], a[156:159], v[52:55], v[180:195]\n"
    "v_cvt_pk_bf16_f32 v96, v80, v81\n"
    "v_cvt_pk_bf16_f32 v97, v82, v83\n"
    "v_cvt_pk_bf16_f32 v98, v84, v85\n"
    "v_cvt_pk_bf16_f32 v99, v86, v87\n"
    "ds_write_b128 v239, v[96:99]\n"
    "v_cvt_pk_bf16_f32 v100, v88, v89\n"
    "v_cvt_pk_bf16_f32 v101, v90, v91\n"
    "v_cvt_pk_bf16_f32 v102, v92, v93\n"
    "v_cvt_pk_bf16_f32 v103, v94, v95\n"
    "ds_write_b128 v239, v[100:103] offset:1024\n"
    "s_xor_b32 m0, m0, 0x400\n"
    "s_nop 0\n"
    "buffer_load_dwordx4 v251, s[32:35], s43 offen lds\n"
    "v_max3_f32 v228, v180, v181, v182\n"
    "v_min3_f32 v229, v180, v181, v182\n"
    "v_max3_f32 v228, v228, v183, v184\n"
    "v_min3_f32 v229, v229, v183, v184\n"
    "v_max3_f32 v228, v228, v185, v186\n"
    "v_min3_f32 v229, v229, v185, v186\n"
    "v_max3_f32 v228, v228, v187, v188\n"
    "v_min3_f32 v229, v229, v187, v188\n"
    "v_max3_f32 v228, v228, v189, v190\n"
    "v_min3_f32 v229, v229, v189, v190\n"
    "v_max3_f32 v228, v228, v191, v192\n"
    "v_min3_f32 v229, v229, v191, v192\n"
    "v_max3_f32 v228, v228, v193, v194\n"
    "v_min3_f32 v229, v229, v193, v194\n"
    "v_max_f32 v228, v228, v195\n"
    "v_min_f32 v229, v229, v195\n"
    "v_pk_mul_f32 v[228:229], v[228:229], s[74:75] op_sel_hi:[1,0] neg_lo:[1,0]\n"
    "v_pk_fma_f32 v[196:197], v[180:181], s[74:75], v[228:229] op_sel_hi:[1,0,0]\n"
    "v_pk_fma_f32 v[212:213], v[180:181], s[74:75], v[228:229] op_sel:[0,0,1] op_sel_hi:[1,0,1] neg_lo:[1,0,0] neg_hi:[1,0,0]\n"
    "v_pk_fma_f32 v[198:199], v[182:183], s[74:75], v[228:229] op_sel_hi:[1,0,0]\n"
    "s_xor_b32 m0, m0, 0x1c00\n"
    "v_pk_fma_f32 v[214:215], v[182:183], s[74:75], v[228:229] op_sel:[0,0,1] op_sel_hi:[1,0,1] neg_lo:[1,0,0] neg_hi:[1,0,0]\n"
    "buffer_load_dwordx4 v251, s[32:35], s48 offen lds\n"
    "v_pk_fma_f32 v[200:201], v[184:185], s[74:75], v[228:229] op_sel_hi:[1,0,0]\n"
    "v_pk_fma_f32 v[216:217], v[184:185], s[74:75], v[228:229] op_sel:[0,0,1] op_sel_hi:[1,0,1] neg_lo:[1,0,0] neg_hi:[1,0,0]\n"
    "v_pk_fma_f32 v[202:203], v[186:187], s[74:75], v[228:229] op_sel_hi:[1,0,0]\n"
    "v_pk_fma_f32 v[218:219], v[186:187], s[74:75], v[228:229] op_sel:[0,0,1] op_sel_hi:[1,0,1] neg_lo:[1,0,0] neg_hi:[1,0,0]\n"
    "v_pk_fma_f32 v[204:205], v[188:189], s[74:75], v[228:229] op_sel_hi:[1,0,0]\n"
    "v_pk_fma_f32 v[220:221], v[188:189], s[74:75], v[228:229] op_sel:[0,0,1] op_sel_hi:[1,0,1] neg_lo:[1,0,0] neg_hi:[1,0,0]\n"
    "v_pk_fma_f32 v[206:207], v[190:191], s[74:75], v[228:229] op_sel_hi:[1,0,0]\n"
    "v_pk_fma_f32 v[222:223], v[190:191], s[74:75], v[228:229] op_sel:[0,0,1] op_sel_hi:[1,0,1] neg_lo:[1,0,0] neg_hi:[1,0,0]\n"
    "v_pk_fma_f32 v[208:209], v[192:193], s[74:75], v[228:229] op_sel_hi:[1,0,0]\n"
    "v_pk_fma_f32 v[224:225], v[192:193], s[74:75], v[228:229] op_sel:[0,0,1] op_sel_hi:[1,0,1] neg_lo:[1,0,0] neg_hi:[1,0,0]\n"
    "v_pk_fma_f32 v[210:211], v[194:195], s[74:75], v[228:229] op_sel_hi:[1,0,0]\n"
    "v_pk_fma_f32 v[226:227], v[194:195], s[74:75], v[228:229] op_sel:[0,0,1] op_sel_hi:[1,0,1] neg_lo:[1,0,0] neg_hi:[1,0,0]\n"
    "v_exp_f32 v196, v196\n"
    "v_exp_f32 v197, v197\n"
    "v_exp_f32 v198, v198\n"
    "v_exp_f32 v199, v199\n"
    "v_exp_f32 v200, v200\n"
    "v_exp_f32 v201, v201\n"
    "v_pk_add_f32 v[230:231], v[196:197], v[198:199]\n"
    "v_exp_f32 v202, v202\n"
    "s_xor_b32 m0, m0, 0x400\n"
    "v_exp_f32 v203, v203\n"
    "buffer_load_dwordx4 v251, s[32:35], s49 offen lds\n"
    "v_pk_add_f32 v[230:231], v[230:231], v[200:201]\n"
    "v_exp_f32 v204, v204\n"
    "v_exp_f32 v205, v205\n"
    "v_pk_add_f32 v[230:231], v[230:231], v[202:203]\n"
    "v_exp_f32 v206, v206\n"
    "v_exp_f32 v207, v207\n"
    "v_pk_add_f32 v[230:231], v[230:231], v[204:205]\n"
    "v_exp_f32 v208, v208\n"
    "v_exp_f32 v209, v209\n"
    "v_pk_add_f32 v[230:231], v[230:231], v[206:207]\n"
    "v_exp_f32 v210, v210\n"
    "v_exp_f32 v211, v211\n"
    "v_pk_add_f32 v[230:231], v[230:231], v[208:209]\n"
    "v_exp_f32 v212, v212\n"
    "v_pk_add_f32 v[230:231], v[230:231], v[210:211]\n"
    "v_add_f32 v228, v230, v231\n"
    "s_xor_b32 m0, m0, 0xc00\n"
    "v_rcp_f32 v228, v228\n"
    "buffer_load_dwordx4 v251, s[32:35], s50 offen lds\n"
    "v_exp_f32 v213, v213\n"
    "v_exp_f32 v214, v214\n"
    "v_exp_f32 v215, v215\n"
    "v_pk_mul_f32 v[196:197], v[196:197], v[228:229] op_sel_hi:[1,0]\n"
    "v_cvt_pk_bf16_f32 v180, v196, v197\n"
    "v_pk_mul_f32 v[198:199], v[198:199], v[228:229] op_sel_hi:[1,0]\n"
    "v_cvt_pk_bf16_f32 v181, v198, v199\n"
    "v_pk_mul_f32 v[200:201], v[200:201], v[228:229] op_sel_hi:[1,0]\n"
    "v_cvt_pk_bf16_f32 v182, v200, v201\n"
    "v_pk_mul_f32 v[202:203], v[202:203], v[228:229] op_sel_hi:[1,0]\n"
    "v_cvt_pk_bf16_f32 v183, v202, v203\n"
    "ds_write_b128 v237, v[180:183]\n"
    "v_exp_f32 v216, v216\n"
    "v_exp_f32 v217, v217\n"
    "s_xor_b32 m0, m0, 0x400\n"
    "v_pk_add_f32 v[232:233], v[212:213], v[214:215]\n"
    "buffer_load_dwordx4 v251, s[32:35], s51 offen lds\n"
    "v_exp_f32 v218, v218\n"
    "v_exp_f32 v219, v219\n"
    "v_pk_add_f32 v[232:233], v[232:233], v[216:217]\n"
    "v_exp_f32 v220, v220\n"
    "v_exp_f32 v221, v221\n"
    "v_pk_add_f32 v[232:233], v[232:233], v[218:219]\n"
    "v_pk_mul_f32 v[204:205], v[204:205], v[228:229] op_sel_hi:[1,0]\n"
    "v_cvt_pk_bf16_f32 v184, v204, v205\n"
    "v_pk_mul_f32 v[206:207], v[206:207], v[228:229] op_sel_hi:[1,0]\n"
    "v_cvt_pk_bf16_f32 v185, v206, v207\n"
    "v_pk_mul_f32 v[208:209], v[208:209], v[228:229] op_sel_hi:[1,0]\n"
    "v_cvt_pk_bf16_f32 v186, v208, v209\n"
    "v_exp_f32 v222, v222\n"
    "v_exp_f32 v223, v223\n"
    "v_pk_add_f32 v[232:233], v[232:233], v[220:221]\n"
    "v_exp_f32 v224, v224\n"
    "v_exp_f32 v225, v225\n"
    "v_pk_add_f32 v[232:233], v[232:233], v[222:223]\n"
    "s_and_b32 s96, s57, 3\n"
    "s_lshl_b32 s96, s96, 1\n"
    "s_lshl_b32 s7, s15, 3\n"
    "s_add_u32 s96, s96, s7\n"
    "s_lshl_b32 s95, s38, 6\n"
    "s_lshl_b32 s88, s96, 10\n"
    "s_add_u32 m0, s88, 0x4000\n"
    "s_mul_i32 s88, s96, 0xac00\n"
    "s_add_u32 s88, s88, s95\n"
    "buffer_load_dwordx4 v249, s[44:47], s88 offen lds\n"
    "v_exp_f32 v226, v226\n"
    "v_exp_f32 v227, v227\n"
    "v_pk_add_f32 v[232:233], v[232:233], v[224:225]\n"
    "v_pk_mul_f32 v[210:211], v[210:211], v[228:229] op_sel_hi:[1,0]\n"
    "v_pk_add_f32 v[232:233], v[232:233], v[226:227]\n"
    "v_add_f32 v229, v232, v233\n"
    "v_rcp_f32 v229, v229\n"
    "v_cvt_pk_bf16_f32 v187, v210, v211\n"
    "s_add_u32 m0, m0, 0x8000\n"
    "s_add_u32 s88, s88, 64\n"
    "buffer_load_dwordx4 v249, s[44:47], s88 offen lds\n"
    "ds_write_b128 v133, v[184:187]\n"
    "v_pk_mul_f32 v[212:213], v[212:213], v[228:229] op_sel:[0,1]\n"
    "v_cvt_pk_bf16_f32 v188, v212, v213\n"
    "v_pk_mul_f32 v[214:215], v[214:215], v[228:229] op_sel:[0,1]\n"
    "v_cvt_pk_bf16_f32 v189, v214, v215\n"
    "v_pk_mul_f32 v[216:217], v[216:217], v[228:229] op_sel:[0,1]\n"
    "v_cvt_pk_bf16_f32 v190, v216, v217\n"
    "v_pk_mul_f32 v[218:219], v[218:219], v[228:229] op_sel:[0,1]\n"
    "v_cvt_pk_bf16_f32 v191, v218, v219\n"
    "ds_write_b128 v134, v[188:191]\n"
    "s_add_u32 m0, m0, 0x8000\n"
    "s_add_u32 s88, s88, 64\n"
    "buffer_load_dwordx4 v250, s[44:47], s88 offen lds\n"
    "v_pk_mul_f32 v[220:221], v[220:221], v[228:229] op_sel:[0,1]\n"
    "v_cvt_pk_bf16_f32 v192, v220, v221\n"
    "v_pk_mul_f32 v[222:223], v[222:223], v[228:229] op_sel:[0,1]\n"
    "v_cvt_pk_bf16_f32 v193, v222, v223\n"
    "v_pk_mul_f32 v[224:225], v[224:225], v[228:229] op_sel:[0,1]\n"
    "v_cvt_pk_bf16_f32 v194, v224, v225\n"
    "v_pk_mul_f32 v[226:227], v[226:227], v[228:229] op_sel:[0,1]\n"
    "v_cvt_pk_bf16_f32 v195, v226, v227\n"
    "ds_write_b128 v135, v[192:195]\n"
    "s_waitcnt vmcnt(3)\n"
    "s_waitcnt lgkmcnt(0)\n"
    "s_barrier\n"
    "ds_read_b64_tr_b16 v[180:181], v238\n"
    "ds_read_b64_tr_b16 v[182:183], v238 offset:512\n"
    "ds_read_b128 v[196:199], v240\n"
    "ds_read_b64_tr_b16 v[184:185], v238 offset:4096\n"
    "ds_read_b64_tr_b16 v[186:187], v238 offset:4608\n"
    "ds_read_b128 v[200:203], v240 offset:2048\n"
    "ds_read_b64_tr_b16 v[188:189], v238 offset:8192\n"
    "ds_read_b64_tr_b16 v[190:191], v238 offset:8704\n"
    "ds_read_b128 v[204:207], v240 offset:4096\n"
    "ds_read_b64_tr_b16 v[192:193], v238 offset:12288\n"
    "ds_read_b64_tr_b16 v[194:195], v238 offset:12800\n"
    "ds_read_b128 v[208:211], v240 offset:6144\n"
    "s_waitcnt lgkmcnt(10)\n"
    "v_mfma_f32_16x16x32_bf16 v[100:103], v[180:183], a[160:163], 0\n"
    "s_waitcnt lgkmcnt(9)\n"
    "v_mfma_f32_16x16x32_bf16 v[96:99], v[180:183], v[196:199], 0\n"
    "s_waitcnt lgkmcnt(7)\n"
    "v_mfma_f32_16x16x32_bf16 v[100:103], v[184:187], a[160:163], v[100:103]\n"
    "s_waitcnt lgkmcnt(6)\n"
    "v_mfma_f32_16x16x32_bf16 v[96:99], v[184:187], v[200:203], v[96:99]\n"
    "s_waitcnt lgkmcnt(4)\n"
    "v_mfma_f32_16x16x32_bf16 v[100:103], v[188:191], a[160:163], v[100:103]\n"
    "s_waitcnt lgkmcnt(3)\n"
    "v_mfma_f32_16x16x32_bf16 v[96:99], v[188:191], v[204:207], v[96:99]\n"
    "s_waitcnt lgkmcnt(1)\n"
    "v_mfma_f32_16x16x32_bf16 v[100:103], v[192:195], a[160:163], v[100:103]\n"
    "s_waitcnt lgkmcnt(0)\n"
    "v_mfma_f32_16x16x32_bf16 v[96:99], v[192:195], v[208:211], v[96:99]\n"
    "ds_read_b64_tr_b16 a[128:129], v235\n"
    "ds_read_b64_tr_b16 a[130:131], v235 offset:256\n"
    "ds_read_b64_tr_b16 a[132:133], v235 offset:1024\n"
    "ds_read_b64_tr_b16 a[134:135], v235 offset:1280\n"
    "s_mov_b32 exec_lo, 0x30003\n"
    "s_mov_b32 exec_hi, 0x30003\n"
    "buffer_store_dwordx4 v[100:103], v241, s[28:31], s68 offen offset:512 sc1\n"
    "s_mov_b64 exec, -1\n"
    "v_cvt_pk_bf16_f32 v96, v96, v97\n"
    "v_cvt_pk_bf16_f32 v97, v98, v99\n"
    "buffer_store_dwordx2 v[96:97], v3, s[28:31], s68 offen sc1\n"
    "ds_read_b64_tr_b16 a[136:137], v235 offset:2048\n"
    "ds_read_b64_tr_b16 a[138:139], v235 offset:2304\n"
    "ds_read_b64_tr_b16 a[140:141], v235 offset:3072\n"
    "ds_read_b64_tr_b16 a[142:143], v235 offset:3328\n"
    "s_waitcnt lgkmcnt(6)\n"
    "v_mfma_f32_32x32x16_bf16 v[64:79], a[128:131], a[0:3], 0\n"
    "ds_read_b64_tr_b16 a[144:145], v235 offset:4096\n"
    "ds_read_b64_tr_b16 a[146:147], v235 offset:4352\n"
    "s_waitcnt lgkmcnt(6)\n"
    "v_mfma_f32_32x32x16_bf16 v[64:79], a[132:135], a[4:7], v[64:79]\n"
    "ds_read_b64_tr_b16 a[148:149], v235 offset:5120\n"
    "ds_read_b64_tr_b16 a[150:151], v235 offset:5376\n"
    "s_waitcnt lgkmcnt(6)\n"
    "v_mfma_f32_32x32x16_bf16 v[64:79], a[136:139], a[8:11], v[64:79]\n"
    "ds_read_b64_tr_b16 a[128:129], v235 offset:6144\n"
    "ds_read_b64_tr_b16 a[130:131], v235 offset:6400\n"
    "s_xor_b32 s96, s96, 1\n"
    "s_lshl_b32 s88, s96, 10\n"
    "s_add_u32 m0, s88, 0x4000\n"
    "s_mul_i32 s88, s96, 0xac00\n"
    "s_add_u32 s88, s88, s95\n"
    "buffer_load_dwordx4 v249, s[44:47], s88 offen lds\n"
    "s_waitcnt lgkmcnt(6)\n"
    "v_mfma_f32_32x32x16_bf16 v[64:79], a[140:143], a[12:15], v[64:79]\n"
    "ds_read_b64_tr_b16 a[132:133], v235 offset:7168\n"
    "ds_read_b64_tr_b16 a[134:135], v235 offset:7424\n"
    "s_waitcnt lgkmcnt(6)\n"
    "v_mfma_f32_32x32x16_bf16 v[64:79], a[144:147], a[16:19], v[64:79]\n"
    "ds_read_b64_tr_b16 a[136:137], v235 offset:8192\n"
    "ds_read_b64_tr_b16 a[138:139], v235 offset:8448\n"
    "s_waitcnt lgkmcnt(6)\n"
    "v_mfma_f32_32x32x16_bf16 v[64:79], a[148:151], a[20:23], v[64:79]\n"
    "ds_read_b64_tr_b16 a[140:141], v235 offset:9216\n"
    "ds_read_b64_tr_b16 a[142:143], v235 offset:9472\n"
    "s_add_u32 m0, m0, 0x8000\n"
    "s_add_u32 s88, s88, 64\n"
    "buffer_load_dwordx4 v249, s[44:47], s88 offen lds\n"
    "s_waitcnt lgkmcnt(6)\n"
    "v_mfma_f32_32x32x16_bf16 v[64:79], a[128:131], a[24:27], v[64:79]\n"
    "ds_read_b64_tr_b16 a[144:145], v235 offset:10240\n"
    "ds_read_b64_tr_b16 a[146:147], v235 offset:10496\n"
    "s_waitcnt lgkmcnt(6)\n"
    "v_mfma_f32_32x32x16_bf16 v[64:79], a[132:135], a[28:31], v[64:79]\n"
    "ds_read_b64_tr_b16 a[148:149], v235 offset:11264\n"
    "ds_read_b64_tr_b16 a[150:151], v235 offset:11520\n"
    "s_waitcnt lgkmcnt(6)\n"
    "v_mfma_f32_32x32x16_bf16 v[64:79], a[136:139], a[32:35], v[64:79]\n"
    "ds_read_b64_tr_b16 a[128:129], v235 offset:12288\n"
    "ds_read_b64_tr_b16 a[130:131], v235 offset:12544\n"
    "s_add_u32 m0, m0, 0x8000\n"
    "s_add_u32 s88, s88, 64\n"
    "buffer_load_dwordx4 v250, s[44:47], s88 offen lds\n"
    "s_waitcnt lgkmcnt(6)\n"
    "v_mfma_f32_32x32x16_bf16 v[64:79], a[140:143], a[36:39], v[64:79]\n"
    "ds_read_b64_tr_b16 a[132:133], v235 offset:13312\n"
    "ds_read_b64_tr_b16 a[134:135], v235 offset:13568\n"
    "s_waitcnt lgkmcnt(6)\n"
    "v_mfma_f32_32x32x16_bf16 v[64:79], a[144:147], a[40:43], v[64:79]\n"
    "ds_read_b64_tr_b16 a[136:137], v235 offset:14336\n"
    "ds_read_b64_tr_b16 a[138:139], v235 offset:14592\n"
    "s_waitcnt lgkmcnt(6)\n"
    "v_mfma_f32_32x32x16_bf16 v[64:79], a[148:151], a[44:47], v[64:79]\n"
    "ds_read_b64_tr_b16 a[140:141], v235 offset:15360\n"
    "ds_read_b64_tr_b16 a[142:143], v235 offset:15616\n"
    "s_xor_b32 s96, s96, 3\n"
    "s_lshl_b32 s88, s96, 10\n"
    "s_add_u32 m0, s88, 0x4000\n"
    "s_mul_i32 s88, s96, 0xac00\n"
    "s_add_u32 s88, s88, s95\n"
    "buffer_load_dwordx4 v249, s[44:47], s88 offen lds\n"
    "s_waitcnt lgkmcnt(6)\n"
    "v_mfma_f32_32x32x16_bf16 v[64:79], a[128:131], a[48:51], v[64:79]\n"
    "ds_read_b64_tr_b16 a[144:145], v235 offset:16384\n"
    "ds_read_b64_tr_b16 a[146:147], v235 offset:16640\n"
    "s_waitcnt vmcnt(4)\n"
    "s_barrier\n"
    "s_cmp_lg_u32 s15, 1\n"
    "s_cbranch_scc1 .Ls6\n"
    "s_mov_b32 exec_hi, 0\n"
    "buffer_store_dwordx2 v[6:7], v19, s[28:31], s63 offen sc1\n"
    "s_mov_b64 exec, -1\n"
    ".Ls6:\n"
    "s_waitcnt lgkmcnt(6)\n"
    "v_mfma_f32_32x32x16_bf16 v[64:79], a[132:135], a[52:55], v[64:79]\n"
    "ds_read_b64_tr_b16 a[148:149], v235 offset:17408\n"
    "ds_read_b64_tr_b16 a[150:151], v235 offset:17664\n"
    "s_waitcnt lgkmcnt(6)\n"
    "v_mfma_f32_32x32x16_bf16 v[64:79], a[136:139], a[56:59], v[64:79]\n"
    "ds_read_b64_tr_b16 a[128:129], v235 offset:18432\n"
    "ds_read_b64_tr_b16 a[130:131], v235 offset:18688\n"
    "s_add_u32 m0, m0, 0x8000\n"
    "s_add_u32 s88, s88, 64\n"
    "buffer_load_dwordx4 v249, s[44:47], s88 offen lds\n"
    "s_waitcnt lgkmcnt(6)\n"
    "v_mfma_f32_32x32x16_bf16 v[64:79], a[140:143], a[60:63], v[64:79]\n"
    "ds_read_b64_tr_b16 a[132:133], v235 offset:19456\n"
    "ds_read_b64_tr_b16 a[134:135], v235 offset:19712\n"
    "s_waitcnt lgkmcnt(6)\n"
    "v_mfma_f32_32x32x16_bf16 v[64:79], a[144:147], a[64:67], v[64:79]\n"
    "ds_read_b64_tr_b16 a[136:137], v235 offset:20480\n"
    "ds_read_b64_tr_b16 a[138:139], v235 offset:20736\n"
    "s_waitcnt lgkmcnt(6)\n"
    "v_mfma_f32_32x32x16_bf16 v[64:79], a[148:151], a[68:71], v[64:79]\n"
    "ds_read_b64_tr_b16 a[140:141], v235 offset:21504\n"
    "ds_read_b64_tr_b16 a[142:143], v235 offset:21760\n"
    "s_add_u32 m0, m0, 0x8000\n"
    "s_add_u32 s88, s88, 64\n"
    "buffer_load_dwordx4 v250, s[44:47], s88 offen lds\n"
    "s_waitcnt lgkmcnt(6)\n"
    "v_mfma_f32_32x32x16_bf16 v[64:79], a[128:131], a[72:75], v[64:79]\n"
    "ds_read_b64_tr_b16 a[144:145], v235 offset:22528\n"
    "ds_read_b64_tr_b16 a[146:147], v235 offset:22784\n"
    "s_waitcnt lgkmcnt(6)\n"
    "v_mfma_f32_32x32x16_bf16 v[64:79], a[132:135], a[76:79], v[64:79]\n"
    "ds_read_b64_tr_b16 a[148:149], v235 offset:23552\n"
    "ds_read_b64_tr_b16 a[150:151], v235 offset:23808\n"
    "s_waitcnt lgkmcnt(6)\n"
    "v_mfma_f32_32x32x16_bf16 v[64:79], a[136:139], a[80:83], v[64:79]\n"
    "ds_read_b64_tr_b16 a[128:129], v235 offset:24576\n"
    "ds_read_b64_tr_b16 a[130:131], v235 offset:24832\n"
    "s_xor_b32 s96, s96, 1\n"
    "s_lshl_b32 s88, s96, 10\n"
    "s_add_u32 m0, s88, 0x4000\n"
    "s_mul_i32 s88, s96, 0xac00\n"
    "s_add_u32 s88, s88, s95\n"
    "buffer_load_dwordx4 v249, s[44:47], s88 offen lds\n"
    "s_waitcnt lgkmcnt(6)\n"
    "v_mfma_f32_32x32x16_bf16 v[64:79], a[140:143], a[84:87], v[64:79]\n"
    "ds_read_b64_tr_b16 a[132:133], v235 offset:25600\n"
    "ds_read_b64_tr_b16 a[134:135], v235 offset:25856\n"
    "s_waitcnt lgkmcnt(6)\n"
    "v_mfma_f32_32x32x16_bf16 v[64:79], a[144:147], a[88:91], v[64:79]\n"
    "ds_read_b64_tr_b16 a[136:137], v235 offset:26624\n"
    "ds_read_b64_tr_b16 a[138:139], v235 offset:26880\n"
    "s_waitcnt lgkmcnt(6)\n"
    "v_mfma_f32_32x32x16_bf16 v[64:79], a[148:151], a[92:95], v[64:79]\n"
    "ds_read_b64_tr_b16 a[140:141], v235 offset:27648\n"
    "ds_read_b64_tr_b16 a[142:143], v235 offset:27904\n"
    "s_add_u32 m0, m0, 0x8000\n"
    "s_add_u32 s88, s88, 64\n"
    "buffer_load_dwordx4 v249, s[44:47], s88 offen lds\n"
    "s_waitcnt lgkmcnt(6)\n"
    "v_mfma_f32_32x32x16_bf16 v[64:79], a[128:131], a[96:99], v[64:79]\n"
    "ds_read_b64_tr_b16 a[144:145], v235 offset:28672\n"
    "ds_read_b64_tr_b16 a[146:147], v235 offset:28928\n"
    "s_waitcnt lgkmcnt(6)\n"
    "v_mfma_f32_32x32x16_bf16 v[64:79], a[132:135], a[100:103], v[64:79]\n"
    "ds_read_b64_tr_b16 a[148:149], v235 offset:29696\n"
    "ds_read_b64_tr_b16 a[150:151], v235 offset:29952\n"
    "s_waitcnt lgkmcnt(6)\n"
    "v_mfma_f32_32x32x16_bf16 v[64:79], a[136:139], a[104:107], v[64:79]\n"
    "ds_read_b64_tr_b16 a[128:129], v235 offset:30720\n"
    "ds_read_b64_tr_b16 a[130:131], v235 offset:30976\n"
    "s_add_u32 m0, m0, 0x8000\n"
    "s_add_u32 s88, s88, 64\n"
    "buffer_load_dwordx4 v250, s[44:47], s88 offen lds\n"
    "s_waitcnt lgkmcnt(6)\n"
    "v_mfma_f32_32x32x16_bf16 v[64:79], a[140:143], a[108:111], v[64:79]\n"
    "ds_read_b64_tr_b16 a[132:133], v235 offset:31744\n"
    "ds_read_b64_tr_b16 a[134:135], v235 offset:32000\n"
    "s_waitcnt lgkmcnt(6)\n"
    "v_mfma_f32_32x32x16_bf16 v[64:79], a[144:147], a[112:115], v[64:79]\n"
    "s_waitcnt lgkmcnt(4)\n"
    "v_mfma_f32_32x32x16_bf16 v[64:79], a[148:151], a[116:119], v[64:79]\n"
    "s_xor_b32 s96, s96, 7\n"
    "s_lshl_b32 s88, s96, 10\n"
    "s_add_u32 m0, s88, 0x4000\n"
    "s_mul_i32 s88, s96, 0xac00\n"
    "s_add_u32 s88, s88, s95\n"
    "buffer_load_dwordx4 v249, s[44:47], s88 offen lds\n"
    "s_waitcnt lgkmcnt(2)\n"
    "v_mfma_f32_32x32x16_bf16 v[64:79], a[128:131], a[120:123], v[64:79]\n"
    "s_waitcnt lgkmcnt(0)\n"
    "v_mfma_f32_32x32x16_bf16 v[64:79], a[132:135], a[124:127], v[64:79]\n"
    "s_nop 11\n"
    "v_cvt_pk_bf16_f32 v48, v64, v65\n"
    "v_cvt_pk_bf16_f32 v49, v66, v67\n"
    "v_cvt_pk_bf16_f32 v50, v68, v69\n"
    "v_cvt_pk_bf16_f32 v51, v70, v71\n"
    "s_nop 1\n"
    "v_mfma_f32_32x32x16_bf16 v[180:195], a[152:155], v[48:51], 0\n"
    "v_cvt_pk_bf16_f32 v52, v72, v73\n"
    "v_cvt_pk_bf16_f32 v53, v74, v75\n"
    "v_cvt_pk_bf16_f32 v54, v76, v77\n"
    "v_cvt_pk_bf16_f32 v55, v78, v79\n"
    "s_nop 1\n"
    "v_mfma_f32_32x32x16_bf16 v[180:195], a[156:159], v[52:55], v[180:195]\n"
    "s_add_u32 m0, m0, 0x8000\n"
    "s_add_u32 s88, s88, 64\n"
    "buffer_load_dwordx4 v249, s[44:47], s88 offen lds\n"
    "s_nop 8\n"
    "v_max3_f32 v228, v180, v181, v182\n"
    "v_min3_f32 v229, v180, v181, v182\n"
    "v_max3_f32 v228, v228, v183, v184\n"
    "v_min3_f32 v229, v229, v183, v184\n"
    "v_max3_f32 v228, v228, v185, v186\n"
    "v_min3_f32 v229, v229, v185, v186\n"
    "v_max3_f32 v228, v228, v187, v188\n"
    "v_min3_f32 v229, v229, v187, v188\n"
    "v_max3_f32 v228, v228, v189, v190\n"
    "v_min3_f32 v229, v229, v189, v190\n"
    "v_max3_f32 v228, v228, v191, v192\n"
    "v_min3_f32 v229, v229, v191, v192\n"
    "v_max3_f32 v228, v228, v193, v194\n"
    "v_min3_f32 v229, v229, v193, v194\n"
    "v_max_f32 v228, v228, v195\n"
    "v_min_f32 v229, v229, v195\n"
    "v_pk_mul_f32 v[228:229], v[228:229], s[74:75] op_sel_hi:[1,0] neg_lo:[1,0]\n"
    "v_pk_fma_f32 v[196:197], v[180:181], s[74:75], v[228:229] op_sel_hi:[1,0,0]\n"
    "v_pk_fma_f32 v[212:213], v[180:181], s[74:75], v[228:229] op_sel:[0,0,1] op_sel_hi:[1,0,1] neg_lo:[1,0,0] neg_hi:[1,0,0]\n"
    "v_pk_fma_f32 v[198:199], v[182:183], s[74:75], v[228:229] op_sel_hi:[1,0,0]\n"
    "v_pk_fma_f32 v[214:215], v[182:183], s[74:75], v[228:229] op_sel:[0,0,1] op_sel_hi:[1,0,1] neg_lo:[1,0,0] neg_hi:[1,0,0]\n"
    "s_add_u32 m0, m0, 0x8000\n"
    "s_add_u32 s88, s88, 64\n"
    "buffer_load_dwordx4 v250, s[44:47], s88 offen lds\n"
    "v_pk_fma_f32 v[200:201], v[184:185], s[74:75], v[228:229] op_sel_hi:[1,0,0]\n"
    "v_pk_fma_f32 v[216:217], v[184:185], s[74:75], v[228:229] op_sel:[0,0,1] op_sel_hi:[1,0,1] neg_lo:[1,0,0] neg_hi:[1,0,0]\n"
    "v_pk_fma_f32 v[202:203], v[186:187], s[74:75], v[228:229] op_sel_hi:[1,0,0]\n"
    "v_pk_fma_f32 v[218:219], v[186:187], s[74:75], v[228:229] op_sel:[0,0,1] op_sel_hi:[1,0,1] neg_lo:[1,0,0] neg_hi:[1,0,0]\n"
    "v_pk_fma_f32 v[204:205], v[188:189], s[74:75], v[228:229] op_sel_hi:[1,0,0]\n"
    "v_pk_fma_f32 v[220:221], v[188:189], s[74:75], v[228:229] op_sel:[0,0,1] op_sel_hi:[1,0,1] neg_lo:[1,0,0] neg_hi:[1,0,0]\n"
    "v_pk_fma_f32 v[206:207], v[190:191], s[74:75], v[228:229] op_sel_hi:[1,0,0]\n"
    "v_pk_fma_f32 v[222:223], v[190:191], s[74:75], v[228:229] op_sel:[0,0,1] op_sel_hi:[1,0,1] neg_lo:[1,0,0] neg_hi:[1,0,0]\n"
    "v_pk_fma_f32 v[208:209], v[192:193], s[74:75], v[228:229] op_sel_hi:[1,0,0]\n"
    "v_pk_fma_f32 v[224:225], v[192:193], s[74:75], v[228:229] op_sel:[0,0,1] op_sel_hi:[1,0,1] neg_lo:[1,0,0] neg_hi:[1,0,0]\n"
    "v_pk_fma_f32 v[210:211], v[194:195], s[74:75], v[228:229] op_sel_hi:[1,0,0]\n"
    "v_pk_fma_f32 v[226:227], v[194:195], s[74:75], v[228:229] op_sel:[0,0,1] op_sel_hi:[1,0,1] neg_lo:[1,0,0] neg_hi:[1,0,0]\n"
    "v_exp_f32 v196, v196\n"
    "v_exp_f32 v197, v197\n"
    "s_cmp_lg_u32 s15, 0\n"
    "s_cbranch_scc1 .Lr7\n"
    "v_not_b32 v31, v7\n"
    "s_mov_b32 exec_hi, 0\n"
    "buffer_load_dwordx2 v[30:31], v3, s[28:31], s65 offen sc1\n"
    "s_mov_b64 exec, -1\n"
    ".Lr7:\n"
    "s_xor_b32 s96, s96, 1\n"
    "s_lshl_b32 s88, s96, 10\n"
    "s_add_u32 m0, s88, 0x4000\n"
    "s_mul_i32 s88, s96, 0xac00\n"
    "s_add_u32 s88, s88, s95\n"
    "buffer_load_dwordx4 v249, s[44:47], s88 offen lds\n"
    "v_exp_f32 v212, v212\n"
    "v_exp_f32 v213, v213\n"
    "v_exp_f32 v198, v198\n"
    "v_exp_f32 v199, v199\n"
    "v_exp_f32 v214, v214\n"
    "v_exp_f32 v215, v215\n"
    "v_exp_f32 v200, v200\n"
    "v_exp_f32 v201, v201\n"
    "v_exp_f32 v216, v216\n"
    "s_add_u32 m0, m0, 0x8000\n"
    "s_add_u32 s88, s88, 64\n"
    "buffer_load_dwordx4 v249, s[44:47], s88 offen lds\n"
    "v_exp_f32 v217, v217\n"
    "v_pk_add_f32 v[230:231], v[196:197], v[198:199]\n"
    "v_pk_add_f32 v[232:233], v[212:213], v[214:215]\n"
    "v_exp_f32 v202, v202\n"
    "v_exp_f32 v203, v203\n"
    "v_exp_f32 v218, v218\n"
    "v_exp_f32 v219, v219\n"
    "v_pk_add_f32 v[230:231], v[230:231], v[200:201]\n"
    "v_pk_add_f32 v[232:233], v[232:233], v[216:217]\n"
    "v_exp_f32 v204, v204\n"
    "v_exp_f32 v205, v205\n"
    "v_exp_f32 v220, v220\n"
    "v_exp_f32 v221, v221\n"
    "s_add_u32 m0, m0, 0x8000\n"
    "s_add_u32 s88, s88, 64\n"
    "buffer_load_dwordx4 v250, s[44:47], s88 offen lds\n"
    "v_pk_add_f32 v[230:231], v[230:231], v[202:203]\n"
    "v_pk_add_f32 v[232:233], v[232:233], v[218:219]\n"
    "v_exp_f32 v206, v206\n"
    "v_exp_f32 v207, v207\n"
    "v_exp_f32 v222, v222\n"
    "v_exp_f32 v223, v223\n"
    "v_pk_add_f32 v[230:231], v[230:231], v[204:205]\n"
    "v_pk_add_f32 v[232:233], v[232:233], v[220:221]\n"
    "v_exp_f32 v208, v208\n"
    "v_exp_f32 v209, v209\n"
    "v_exp_f32 v224, v224\n"
    "v_exp_f32 v225, v225\n"
    "v_pk_add_f32 v[230:231], v[230:231], v[206:207]\n"
    "s_xor_b32 s96, s96, 3\n"
    "s_lshl_b32 s88, s96, 10\n"
    "s_add_u32 m0, s88, 0x4000\n"
    "s_mul_i32 s88, s96, 0xac00\n"
    "s_add_u32 s88, s88, s95\n"
    "buffer_load_dwordx4 v249, s[44:47], s88 offen lds\n"
    "v_pk_add_f32 v[232:233], v[232:233], v[222:223]\n"
    "v_exp_f32 v210, v210\n"
    "v_exp_f32 v211, v211\n"
    "v_exp_f32 v226, v226\n"
    "v_exp_f32 v227, v227\n"
    "v_pk_add_f32 v[230:231], v[230:231], v[208:209]\n"
    "v_pk_add_f32 v[232:233], v[232:233], v[224:225]\n"
    "v_pk_add_f32 v[230:231], v[230:231], v[210:211]\n"
    "v_pk_add_f32 v[232:233], v[232:233], v[226:227]\n"
    "v_add_f32 v228, v230, v231\n"
    "v_add_f32 v229, v232, v233\n"
    "v_rcp_f32 v228, v228\n"
    "v_rcp_f32 v229, v229\n"
    "s_nop 0\n"
    "v_pk_mul_f32 v[196:197], v[196:197], v[228:229] op_sel_hi:[1,0]\n"
    "v_cvt_pk_bf16_f32 v180, v196, v197\n"
    "v_pk_mul_f32 v[198:199], v[198:199], v[228:229] op_sel_hi:[1,0]\n"
    "v_cvt_pk_bf16_f32 v181, v198, v199\n"
    "v_pk_mul_f32 v[200:201], v[200:201], v[228:229] op_sel_hi:[1,0]\n"
    "v_cvt_pk_bf16_f32 v182, v200, v201\n"
    "v_pk_mul_f32 v[202:203], v[202:203], v[228:229] op_sel_hi:[1,0]\n"
    "v_cvt_pk_bf16_f32 v183, v202, v203\n"
    "v_pk_mul_f32 v[204:205], v[204:205], v[228:229] op_sel_hi:[1,0]\n"
    "v_cvt_pk_bf16_f32 v184, v204, v205\n"
    "v_pk_mul_f32 v[206:207], v[206:207], v[228:229] op_sel_hi:[1,0]\n"
    "v_cvt_pk_bf16_f32 v185, v206, v207\n"
    "v_pk_mul_f32 v[208:209], v[208:209], v[228:229] op_sel_hi:[1,0]\n"
    "v_cvt_pk_bf16_f32 v186, v208, v209\n"
    "v_pk_mul_f32 v[210:211], v[210:211], v[228:229] op_sel_hi:[1,0]\n"
    "v_cvt_pk_bf16_f32 v187, v210, v211\n"
    "v_pk_mul_f32 v[212:213], v[212:213], v[228:229] op_sel:[0,1]\n"
    "v_cvt_pk_bf16_f32 v188, v212, v213\n"
    "v_pk_mul_f32 v[214:215], v[214:215], v[228:229] op_sel:[0,1]\n"
    "v_cvt_pk_bf16_f32 v189, v214, v215\n"
    "v_pk_mul_f32 v[216:217], v[216:217], v[228:229] op_sel:[0,1]\n"
    "v_cvt_pk_bf16_f32 v190, v216, v217\n"
    "v_pk_mul_f32 v[218:219], v[218:219], v[228:229] op_sel:[0,1]\n"
    "v_cvt_pk_bf16_f32 v191, v218, v219\n"
    "v_pk_mul_f32 v[220:221], v[220:221], v[228:229] op_sel:[0,1]\n"
    "v_cvt_pk_bf16_f32 v192, v220, v221\n"
    "v_pk_mul_f32 v[222:223], v[222:223], v[228:229] op_sel:[0,1]\n"
    "v_cvt_pk_bf16_f32 v193, v222, v223\n"
    "v_pk_mul_f32 v[224:225], v[224:225], v[228:229] op_sel:[0,1]\n"
    "v_cvt_pk_bf16_f32 v194, v224, v225\n"
    "v_pk_mul_f32 v[226:227], v[226:227], v[228:229] op_sel:[0,1]\n"
    "v_cvt_pk_bf16_f32 v195, v226, v227\n"
    "s_cmp_lg_u32 s15, 0\n"
    "s_cbranch_scc1 .Lr8\n"
    "s_mov_b32 exec_hi, 0\n"
    "s_waitcnt vmcnt(4)\n"
    "v_cmp_ne_u64 vcc, s[12:13], v[30:31]\n"
    "s_cbranch_vccz .Lq8\n"
    ".Lp8:\n"
    "buffer_load_dwordx2 v[30:31], v3, s[28:31], s65 offen sc1\n"
    "s_sub_i32 s14, s14, 1\n"
    "s_cmp_lt_i32 s14, 0\n"
    "s_waitcnt vmcnt(0)\n"
    "s_cbranch_scc1 .Lq8\n"
    "v_cmp_ne_u64 vcc, s[12:13], v[30:31]\n"
    "s_cbranch_vccnz .Lp8\n"
    ".Lq8:\n"
    "s_mov_b64 exec, -1\n"
    ".Lr8:\n"
    "s_barrier\n"
    "buffer_load_dwordx4 v[96:99], v244, s[28:31], s69 offen offset:512 sc1\n"
    "v_accvgpr_write_b32 a128, 0\n"
    "v_accvgpr_write_b32 a129, 0\n"
    "v_accvgpr_write_b32 a130, 0\n"
    "v_accvgpr_write_b32 a131, 0\n"
    "v_accvgpr_write_b32 a132, 0\n"
    "v_accvgpr_write_b32 a133, 0\n"
    "v_accvgpr_write_b32 a134, 0\n"
    "buffer_load_dwordx4 v[64:67], v243, s[28:31], s69 offen sc1\n"
    "v_accvgpr_write_b32 a135, 0\n"
    "v_accvgpr_write_b32 a136, 0\n"
    "v_accvgpr_write_b32 a137, 0\n"
    "v_accvgpr_write_b32 a138, 0\n"
    "v_accvgpr_write_b32 a139, 0\n"
    "v_accvgpr_write_b32 a140, 0\n"
    "v_accvgpr_write_b32 a141, 0\n"
    "s_add_u32 s69, s69, 0x1400\n"
    "buffer_load_dwordx4 v[68:71], v243, s[28:31], s69 offen sc1\n"
    "v_accvgpr_write_b32 a142, 0\n"
    "v_accvgpr_write_b32 a143, 0\n"
    "v_accvgpr_write_b32 a144, 0\n"
    "v_accvgpr_write_b32 a145, 0\n"
    "v_accvgpr_write_b32 a146, 0\n"
    "v_accvgpr_write_b32 a147, 0\n"
    "s_add_u32 s69, s69, 0x1400\n"
    "buffer_load_dwordx4 v[72:75], v243, s[28:31], s69 offen sc1\n"
    "v_accvgpr_write_b32 a148, 0\n"
    "v_accvgpr_write_b32 a149, 0\n"
    "v_accvgpr_write_b32 a150, 0\n"
    "v_accvgpr_write_b32 a151, 0\n"
    "v_accvgpr_write_b32 a152, 0\n"
    "v_accvgpr_write_b32 a153, 0\n"
    "s_add_u32 s69, s69, 0x1400\n"
    "buffer_load_dwordx4 v[76:79], v243, s[28:31], s69 offen sc1\n"
    "v_accvgpr_write_b32 a154, 0\n"
    "v_accvgpr_write_b32 a155, 0\n"
    "v_accvgpr_write_b32 a156, 0\n"
    "v_accvgpr_write_b32 a157, 0\n"
    "v_accvgpr_write_b32 a158, 0\n"
    "v_accvgpr_write_b32 a159, 0\n"
    "s_add_u32 s69, s69, 0x1400\n"
    "buffer_load_dwordx4 v[80:83], v243, s[28:31], s69 offen sc1\n"
    "v_mov_b32 v100, 0\n"
    "v_mov_b32 v101, 0\n"
    "v_mov_b32 v102, 0\n"
    "v_mov_b32 v103, 0\n"
    "v_mov_b32 v104, 0\n"
    "v_mov_b32 v105, 0\n"
    "s_add_u32 s69, s69, 0x1400\n"
    "buffer_load_dwordx4 v[84:87], v243, s[28:31], s69 offen sc1\n"
    "v_mov_b32 v106, 0\n"
    "v_mov_b32 v107, 0\n"
    "s_mov_b32 s4, 0x3f80\n"
    "s_mov_b32 s5, 0x3f800000\n"
    "s_nop 1\n"
    "s_add_u32 s69, s69, 0x1400\n"
    "buffer_load_dwordx4 v[88:91], v243, s[28:31], s69 offen sc1\n"
    "s_nop 5\n"
    "s_add_u32 s69, s69, 0x1400\n"
    "buffer_load_dwordx4 v[92:95], v243, s[28:31], s69 offen sc1\n"
    "s_lshl_b32 s6, s15, 11\n"
    "s_lshl_b32 s7, s60, 13\n"
    "s_add_u32 s6, s6, s7\n"
    "s_add_u32 s6, s6, 0x60000\n"
    "buffer_load_dwordx4 v[136:139], v242, s[28:31], s6 offen sc1\n"
    "s_add_u32 m0, m0, 0x8000\n"
    "s_add_u32 s88, s88, 64\n"
    "buffer_load_dwordx4 v249, s[44:47], s88 offen lds\n"
    "s_add_u32 m0, m0, 0x8000\n"
    "s_add_u32 s88, s88, 64\n"
    "buffer_load_dwordx4 v250, s[44:47], s88 offen lds\n"
    "s_xor_b32 s96, s96, 1\n"
    "s_lshl_b32 s88, s96, 10\n"
    "s_add_u32 m0, s88, 0x4000\n"
    "s_mul_i32 s88, s96, 0xac00\n"
    "s_add_u32 s88, s88, s95\n"
    "buffer_load_dwordx4 v249, s[44:47], s88 offen lds\n"
    "s_add_u32 m0, m0, 0x8000\n"
    "s_add_u32 s88, s88, 64\n"
    "buffer_load_dwordx4 v249, s[44:47], s88 offen lds\n"
    "s_waitcnt vmcnt(13)\n"
    "v_permlane32_swap_b32 v96, v98\n"
    "v_permlane32_swap_b32 v97, v99\n"
    "v_add_f32 v96, v96, v98\n"
    "v_add_f32 v97, v97, v99\n"
    "s_waitcnt vmcnt(12)\n"
    "v_dot2c_f32_bf16 v100, s4, v64\n"
    "v_dot2c_f32_bf16 v101, s5, v64\n"
    "v_dot2c_f32_bf16 v102, s4, v65\n"
    "v_dot2c_f32_bf16 v103, s5, v65\n"
    "v_dot2c_f32_bf16 v104, s4, v66\n"
    "v_dot2c_f32_bf16 v105, s5, v66\n"
    "v_dot2c_f32_bf16 v106, s4, v67\n"
    "v_dot2c_f32_bf16 v107, s5, v67\n"
    "v_permlane16_swap_b32 v96, v97\n"
    "v_add_f32 v96, v96, v97\n"
    "s_waitcnt vmcnt(11)\n"
    "v_dot2c_f32_bf16 v100, s4, v68\n"
    "v_dot2c_f32_bf16 v101, s5, v68\n"
    "v_dot2c_f32_bf16 v102, s4, v69\n"
    "v_dot2c_f32_bf16 v103, s5, v69\n"
    "v_dot2c_f32_bf16 v104, s4, v70\n"
    "v_dot2c_f32_bf16 v105, s5, v70\n"
    "v_dot2c_f32_bf16 v106, s4, v71\n"
    "v_dot2c_f32_bf16 v107, s5, v71\n"
    "v_add_f32_dpp v96, v96, v96 row_ror:8 row_mask:0xf bank_mask:0xf\n"
    "s_waitcnt vmcnt(10)\n"
    "v_dot2c_f32_bf16 v100, s4, v72\n"
    "v_dot2c_f32_bf16 v101, s5, v72\n"
    "s_add_u32 m0, m0, 0x8000\n"
    "s_add_u32 s88, s88, 64\n"
    "buffer_load_dwordx4 v250, s[44:47], s88 offen lds\n"
    "v_dot2c_f32_bf16 v102, s4, v73\n"
    "v_dot2c_f32_bf16 v103, s5, v73\n"
    "v_dot2c_f32_bf16 v104, s4, v74\n"
    "v_dot2c_f32_bf16 v105, s5, v74\n"
    "v_dot2c_f32_bf16 v106, s4, v75\n"
    "v_dot2c_f32_bf16 v107, s5, v75\n"
    "v_add_f32_dpp v96, v96, v96 row_ror:4 row_mask:0xf bank_mask:0xf\n"
    "v_cvt_pk_bf16_f32 v96, v96, v96\n"
    "ds_write_b16 v246, v96 offset:7168\n"
    "s_waitcnt vmcnt(10)\n"
    "v_dot2c_f32_bf16 v100, s4, v76\n"
    "v_dot2c_f32_bf16 v101, s5, v76\n"
    "v_dot2c_f32_bf16 v102, s4, v77\n"
    "v_dot2c_f32_bf16 v103, s5, v77\n"
    "v_dot2c_f32_bf16 v104, s4, v78\n"
    "v_dot2c_f32_bf16 v105, s5, v78\n"
    "v_dot2c_f32_bf16 v106, s4, v79\n"
    "v_dot2c_f32_bf16 v107, s5, v79\n"
    "s_waitcnt lgkmcnt(0)\n"
    "s_barrier\n"
    "s_mov_b32 exec_lo, 0xf0f0f0f\n"
    "s_mov_b32 exec_hi, 0xf0f0f0f0\n"
    "ds_read_b128 a[144:147], v248 offset:7168\n"
    "ds_read_b128 a[148:151], v248 offset:7184\n"
    "ds_read_b128 a[152:155], v248 offset:7200\n"
    "ds_read_b128 a[156:159], v248 offset:7216\n"
    "s_mov_b64 exec, -1\n"
    "buffer_load_dwordx4 a[176:179], v12, s[28:31], s71 offen sc1\n"
    "s_waitcnt vmcnt(10)\n"
    "v_dot2c_f32_bf16 v100, s4, v80\n"
    "v_dot2c_f32_bf16 v101, s5, v80\n"
    "v_dot2c_f32_bf16 v102, s4, v81\n"
    "v_dot2c_f32_bf16 v103, s5, v81\n"
    "v_dot2c_f32_bf16 v104, s4, v82\n"
    "v_dot2c_f32_bf16 v105, s5, v82\n"
    "v_dot2c_f32_bf16 v106, s4, v83\n"
    "v_dot2c_f32_bf16 v107, s5, v83\n"
    "s_waitcnt lgkmcnt(3)\n"
    "v_mfma_f32_32x32x16_bf16 v[112:127], a[144:147], v[180:183], 0\n"
    "s_waitcnt lgkmcnt(2)\n"
    "v_mfma_f32_32x32x16_bf16 v[112:127], a[148:151], v[184:187], v[112:127]\n"
    "buffer_load_dwordx4 a[180:183], v12, s[28:31], s71 offen offset:1024 sc1\n"
    "s_waitcnt vmcnt(10)\n"
    "v_dot2c_f32_bf16 v100, s4, v84\n"
    "v_dot2c_f32_bf16 v101, s5, v84\n"
    "v_dot2c_f32_bf16 v102, s4, v85\n"
    "v_dot2c_f32_bf16 v103, s5, v85\n"
    "v_dot2c_f32_bf16 v104, s4, v86\n"
    "v_dot2c_f32_bf16 v105, s5, v86\n"
    "v_dot2c_f32_bf16 v106, s4, v87\n"
    "v_dot2c_f32_bf16 v107, s5, v87\n"
    "s_waitcnt lgkmcnt(1)\n"
    "v_mfma_f32_32x32x16_bf16 v[112:127], a[152:155], v[188:191], v[112:127]\n"
    "s_waitcnt lgkmcnt(0)\n"
    "v_mfma_f32_32x32x16_bf16 v[112:127], a[156:159], v[192:195], v[112:127]\n"
    "s_waitcnt vmcnt(9)\n"
    "v_dot2c_f32_bf16 v100, s4, v88\n"
    "v_dot2c_f32_bf16 v101, s5, v88\n"
    "v_dot2c_f32_bf16 v102, s4, v89\n"
    "v_dot2c_f32_bf16 v103, s5, v89\n"
    "v_dot2c_f32_bf16 v104, s4, v90\n"
    "v_dot2c_f32_bf16 v105, s5, v90\n"
    "v_dot2c_f32_bf16 v106, s4, v91\n"
    "v_dot2c_f32_bf16 v107, s5, v91\n"
    "s_waitcnt vmcnt(8)\n"
    "v_dot2c_f32_bf16 v100, s4, v92\n"
    "v_dot2c_f32_bf16 v101, s5, v92\n"
    "v_dot2c_f32_bf16 v102, s4, v93\n"
    "v_dot2c_f32_bf16 v103, s5, v93\n"
    "v_dot2c_f32_bf16 v104, s4, v94\n"
    "v_dot2c_f32_bf16 v105, s5, v94\n"
    "v_dot2c_f32_bf16 v106, s4, v95\n"
    "v_dot2c_f32_bf16 v107, s5, v95\n"
    "v_permlane32_swap_b32 v100, v104\n"
    "v_permlane32_swap_b32 v101, v105\n"
    "v_permlane32_swap_b32 v102, v106\n"
    "v_permlane32_swap_b32 v103, v107\n"
    "v_pk_add_f32 v[100:101], v[100:101], v[104:105]\n"
    "v_pk_add_f32 v[102:103], v[102:103], v[106:107]\n"
    "v_cvt_pk_bf16_f32 v100, v100, v101\n"
    "v_cvt_pk_bf16_f32 v101, v102, v103\n"
    "ds_write_b64 v245, v[100:101] offset:5120\n"
    "s_waitcnt lgkmcnt(0)\n"
    "s_barrier\n"
    "s_mov_b32 exec_lo, 0xf0f0f0f\n"
    "s_mov_b32 exec_hi, 0xf0f0f0f0\n"
    "ds_read_b128 a[128:131], v247 offset:5120\n"
    "ds_read_b128 a[132:135], v247 offset:5632\n"
    "ds_read_b128 a[136:139], v247 offset:6144\n"
    "ds_read_b128 a[140:143], v247 offset:6656\n"
    "s_mov_b64 exec, -1\n"
    "v_add_f32 v112, 0x358637bd, v112\n"
    "v_rcp_f32 v112, v112\n"
    "s_waitcnt lgkmcnt(3)\n"
    "v_mfma_f32_32x32x16_bf16 v[64:79], a[128:131], v[180:183], 0\n"
    "s_waitcnt lgkmcnt(2)\n"
    "v_mfma_f32_32x32x16_bf16 v[64:79], a[132:135], v[184:187], v[64:79]\n"
    "s_waitcnt lgkmcnt(1)\n"
    "v_mfma_f32_32x32x16_bf16 v[64:79], a[136:139], v[188:191], v[64:79]\n"
    "s_waitcnt lgkmcnt(0)\n"
    "v_mfma_f32_32x32x16_bf16 v[64:79], a[140:143], v[192:195], v[64:79]\n"
    "s_nop 11\n"
    "v_pk_mul_f32 v[64:65], v[64:65], v[112:113] op_sel_hi:[1,0]\n"
    "v_cvt_pk_bf16_f32 v96, v64, v65\n"
    "v_pk_mul_f32 v[66:67], v[66:67], v[112:113] op_sel_hi:[1,0]\n"
    "v_cvt_pk_bf16_f32 v97, v66, v67\n"
    "v_pk_mul_f32 v[68:69], v[68:69], v[112:113] op_sel_hi:[1,0]\n"
    "v_cvt_pk_bf16_f32 v98, v68, v69\n"
    "v_pk_mul_f32 v[70:71], v[70:71], v[112:113] op_sel_hi:[1,0]\n"
    "v_cvt_pk_bf16_f32 v99, v70, v71\n"
    "buffer_store_dwordx4 v[96:99], v2, s[28:31], s70 offen sc1\n"
    "v_pk_mul_f32 v[72:73], v[72:73], v[112:113] op_sel_hi:[1,0]\n"
    "v_cvt_pk_bf16_f32 v100, v72, v73\n"
    "v_pk_mul_f32 v[74:75], v[74:75], v[112:113] op_sel_hi:[1,0]\n"
    "v_cvt_pk_bf16_f32 v101, v74, v75\n"
    "v_pk_mul_f32 v[76:77], v[76:77], v[112:113] op_sel_hi:[1,0]\n"
    "v_cvt_pk_bf16_f32 v102, v76, v77\n"
    "v_pk_mul_f32 v[78:79], v[78:79], v[112:113] op_sel_hi:[1,0]\n"
    "v_cvt_pk_bf16_f32 v103, v78, v79\n"
    "buffer_store_dwordx4 v[100:103], v2, s[28:31], s70 offen offset:1024 sc1\n"
    "s_lshl_b32 s6, s15, 10\n"
    "v_add_u32 v10, s6, v2\n"
    "s_waitcnt vmcnt(9)\n"
    "ds_write_b128 v10, v[136:139] offset:8192\n"
    "buffer_load_dwordx4 a[184:187], v12, s[28:31], s71 offen offset:2048 sc1\n"
    "v_lshlrev_b32 v14, 5, v4\n"
    "v_lshlrev_b32 v8, 3, v27\n"
    "v_lshl_or_b32 v8, v129, 5, v8\n"
    "v_lshl_or_b32 v8, v128, 6, v8\n"
    "v_lshl_or_b32 v8, v5, 9, v8\n"
    "v_add_u32 v8, 0x4000, v8\n"
    "v_add_u32 v9, 0x10000, v8\n"
    "v_lshlrev_b32 v132, 10, v131\n"
    "s_lshl_b32 s4, s15, 8\n"
    "s_add_u32 s4, s4, 0x2800\n"
    "v_lshl_add_u32 v25, v5, 4, s4\n"
    "v_sub_u32 v25, v25, v132\n"
    "s_lshl_b32 s4, s15, 3\n"
    "v_add_u32 v26, s4, v14\n"
    "s_cmp_eq_u32 s15, 3\n"
    "s_cselect_b32 s4, 0x80000000, 0\n"
    "buffer_load_dwordx4 a[188:191], v12, s[28:31], s71 offen offset:3072 sc1\n"
    "v_or_b32 v21, s4, v12\n"
    "s_xor_b32 s5, s15, 1\n"
    "s_mul_i32 s5, s5, 0x3000\n"
    "s_add_u32 s5, s5, 0x1c000\n"
    "v_add_u32 v15, s5, v2\n"
    "s_xor_b32 s5, s15, 2\n"
    "s_mul_i32 s5, s5, 0x3000\n"
    "s_add_u32 s5, s5, 0x1d000\n"
    "v_add_u32 v16, s5, v2\n"
    "s_xor_b32 s5, s15, 3\n"
    "s_mul_i32 s5, s5, 0x3000\n"
    "s_add_u32 s5, s5, 0x1e000\n"
    "v_add_u32 v17, s5, v2\n"
    "s_mul_i32 s5, s15, 0x3000\n"
    "s_add_u32 s5, s5, 0x1c000\n"
    "s_xor_b32 s71, s71, 0x1000\n"
    "buffer_load_dwordx4 a[192:195], v12, s[28:31], s71 offen sc1\n"
    "s_nop 13\n"
    "buffer_load_dwordx4 a[196:199], v12, s[28:31], s71 offen offset:1024 sc1\n"
    "s_nop 14\n"
    "buffer_load_dwordx4 a[200:203], v12, s[28:31], s71 offen offset:2048 sc1\n"
    "s_nop 14\n"
    "buffer_load_dwordx4 a[204:207], v12, s[28:31], s71 offen offset:3072 sc1\n"
    "s_waitcnt lgkmcnt(0)\n"
    "s_waitcnt vmcnt(6)\n"
    "s_barrier\n"
    "s_cmp_lg_u32 s15, 1\n"
    "s_cbranch_scc1 .Ls9\n"
    "s_mov_b64 exec, 0xffff\n"
    "buffer_store_dwordx2 v[6:7], v19, s[28:31], s62 offen sc1\n"
    "s_mov_b64 exec, -1\n"
    ".Ls9:\n"
    "s_add_u32 s62, s62, 0x20000\n"
    "s_lshl_b32 s6, s56, 2\n"
    "s_add_u32 s6, s6, s15\n"
    "s_cmp_lg_u32 s6, 3\n"
    "s_cbranch_scc1 .Lnoepoch\n"
    "s_mov_b64 exec, 0x1\n"
    "buffer_store_dwordx2 v[6:7], off, s[28:31], 0  sc1\n"
    "s_mov_b64 exec, -1\n"
    ".Lnoepoch:\n"
    "ds_read_b128 v[224:227], v13\n"
    "ds_read_b128 v[228:231], v13 offset:32\n"
    "ds_read_b128 v[64:67], v13 offset:2048\n"
    "ds_read_b128 v[68:71], v13 offset:2080\n"
    "s_xor_b32 s71, s71, 0x3000\n"
    "buffer_load_dwordx4 a[208:211], v12, s[28:31], s71 offen sc1\n"
    "v_add_u32 v18, s5, v2\n"
    "s_lshl_b32 s77, s60, 18\n"
    "s_mul_i32 s7, s15, 0x3000\n"
    "s_add_u32 s77, s77, s7\n"
    "s_add_u32 s77, s77, 0xe30000\n"
    "s_lshl_b32 s82, s61, 15\n"
    "s_add_u32 s82, s82, 0x600000\n"
    "s_lshl_b32 s83, s61, 12\n"
    "s_add_u32 s83, s83, 0x1c0000\n"
    "s_mul_i32 s84, s61, 0x15800\n"
    "s_lshl_b32 s7, s58, 12\n"
    "s_add_u32 s84, s84, s7\n"
    "s_add_u32 s84, s84, 0x800000\n"
    "s_mul_i32 s85, s61, 0x15800\n"
    "s_lshl_b32 s7, s58, 11\n"
    "s_add_u32 s85, s85, s7\n"
    "s_add_u32 s85, s85, 0x810000\n"
    "buffer_load_dwordx4 a[212:215], v12, s[28:31], s71 offen offset:1024 sc1\n"
    "s_add_u32 s80, s80, 0x400000\n"
    "s_mul_i32 s4, s15, 3\n"
    "s_lshl_b32 s37, 7, s4\n"
    "s_lshl_b32 s4, s59, 1\n"
    "s_add_u32 s4, s4, s2\n"
    "s_xor_b32 s5, s4, 1\n"
    "s_lshl_b32 s79, s4, 15\n"
    "s_lshl_b32 s7, s15, 13\n"
    "s_add_u32 s79, s79, s7\n"
    "s_add_u32 s79, s79, 0x400000\n"
    "s_lshl_b32 s78, s5, 15\n"
    "s_lshl_b32 s7, s15, 13\n"
    "s_add_u32 s78, s78, s7\n"
    "s_add_u32 s78, s78, 0x400000\n"
    "s_mul_i32 s17, s4, 0x15800\n"
    "s_lshl_b32 s7, s15, 14\n"
    "s_add_u32 s17, s17, s7\n"
    "s_add_u32 s17, s17, 0x800000\n"
    "buffer_load_dwordx4 a[216:219], v12, s[28:31], s71 offen offset:2048 sc1\n"
    "s_mul_i32 s16, s5, 0x15800\n"
    "s_lshl_b32 s7, s15, 14\n"
    "s_add_u32 s16, s16, s7\n"
    "s_add_u32 s16, s16, 0x800000\n"
    "s_mul_i32 s19, s4, 0x15800\n"
    "s_mul_i32 s7, s15, 0x1800\n"
    "s_add_u32 s19, s19, s7\n"
    "s_add_u32 s19, s19, 0x810000\n"
    "s_mul_i32 s18, s5, 0x15800\n"
    "s_mul_i32 s7, s15, 0x1800\n"
    "s_add_u32 s18, s18, s7\n"
    "s_add_u32 s18, s18, 0x810000\n"
    "s_waitcnt lgkmcnt(3)\n"
    "v_lshlrev_b32 v192, 16, v224\n"
    "v_lshlrev_b32 v193, 16, v225\n"
    "v_lshlrev_b32 v194, 16, v226\n"
    "buffer_load_dwordx4 a[220:223], v12, s[28:31], s71 offen offset:3072 sc1\n"
    "v_lshlrev_b32 v195, 16, v227\n"
    "v_and_b32 v196, s75, v224\n"
    "v_and_b32 v197, s75, v225\n"
    "v_and_b32 v198, s75, v226\n"
    "v_and_b32 v199, s75, v227\n"
    "s_waitcnt lgkmcnt(2)\n"
    "v_lshlrev_b32 v200, 16, v228\n"
    "v_lshlrev_b32 v201, 16, v229\n"
    "v_lshlrev_b32 v202, 16, v230\n"
    "v_lshlrev_b32 v203, 16, v231\n"
    "v_and_b32 v204, s75, v228\n"
    "v_and_b32 v205, s75, v229\n"
    "v_and_b32 v206, s75, v230\n"
    "v_and_b32 v207, s75, v231\n"
    "s_waitcnt lgkmcnt(1)\n"
    "v_lshlrev_b32 v208, 16, v64\n"
    "v_lshlrev_b32 v209, 16, v65\n"
    "s_xor_b32 s71, s71, 0x1000\n"
    "buffer_load_dwordx4 a[224:227], v12, s[28:31], s71 offen sc1\n"
    "v_lshlrev_b32 v210, 16, v66\n"
    "v_lshlrev_b32 v211, 16, v67\n"
    "v_and_b32 v212, s75, v64\n"
    "v_and_b32 v213, s75, v65\n"
    "v_and_b32 v214, s75, v66\n"
    "v_and_b32 v215, s75, v67\n"
    "s_waitcnt lgkmcnt(0)\n"
    "v_lshlrev_b32 v216, 16, v68\n"
    "v_lshlrev_b32 v217, 16, v69\n"
    "v_lshlrev_b32 v218, 16, v70\n"
    "v_lshlrev_b32 v219, 16, v71\n"
    "v_and_b32 v220, s75, v68\n"
    "v_and_b32 v221, s75, v69\n"
    "v_and_b32 v222, s75, v70\n"
    "v_and_b32 v223, s75, v71\n"
    "s_sleep 2\n"
    "s_cmp_lg_u32 s15, 0\n"
    "s_cbranch_scc1 .Lr10\n"
    "v_not_b32 v31, v7\n"
    "s_mov_b64 exec, 0xffff\n"
    "buffer_load_dwordx2 v[30:31], v3, s[28:31], s64 offen sc1\n"
    "s_mov_b64 exec, -1\n"
    ".Lr10:\n"
    "buffer_load_dwordx4 a[228:231], v12, s[28:31], s71 offen offset:1024 sc1\n"
    "buffer_load_dwordx4 a[232:235], v12, s[28:31], s71 offen offset:2048 sc1\n"
    "buffer_load_dwordx4 a[236:239], v12, s[28:31], s71 offen offset:3072 sc1\n"
    "s_cmp_lg_u32 s15, 0\n"
    "s_cbranch_scc1 .Lr11\n"
    "s_mov_b64 exec, 0xffff\n"
    "s_waitcnt vmcnt(3)\n"
    "v_cmp_ne_u64 vcc, s[12:13], v[30:31]\n"
    "s_cbranch_vccz .Lq11\n"
    ".Lp11:\n"
    "buffer_load_dwordx2 v[30:31], v3, s[28:31], s64 offen sc1\n"
    "s_sub_i32 s14, s14, 1\n"
    "s_cmp_lt_i32 s14, 0\n"
    "s_waitcnt vmcnt(0)\n"
    "s_cbranch_scc1 .Lq11\n"
    "v_cmp_ne_u64 vcc, s[12:13], v[30:31]\n"
    "s_cbranch_vccnz .Lp11\n"
    ".Lq11:\n"
    "s_mov_b64 exec, -1\n"
    ".Lr11:\n"
    "s_barrier\n"
    "s_add_u32 s64, s64, 0x20000\n"
    "buffer_load_dwordx4 v[128:131], v2, s[28:31], s78 offen sc1\n"
    "s_nop 10\n"
    "buffer_load_dwordx4 v[132:135], v2, s[28:31], s78 offen offset:1024 sc1\n"
    "s_nop 10\n"
    "buffer_load_dwordx4 v[136:139], v2, s[28:31], s78 offen offset:2048 sc1\n"
    "s_nop 10\n"
    "buffer_load_dwordx4 v[140:143], v2, s[28:31], s78 offen offset:3072 sc1\n"
    "s_nop 10\n"
    "s_xor_b32 s78, s78, 0x1000\n"
    "buffer_load_dwordx4 v[144:147], v2, s[28:31], s78 offen sc1\n"
    "s_nop 9\n"
    "buffer_load_dwordx4 v[148:151], v2, s[28:31], s78 offen offset:1024 sc1\n"
    "s_nop 10\n"
    "buffer_load_dwordx4 v[152:155], v2, s[28:31], s78 offen offset:2048 sc1\n"
    "s_nop 10\n"
    "buffer_load_dwordx4 v[156:159], v2, s[28:31], s78 offen offset:3072 sc1\n"
    "s_nop 10\n"
    "buffer_load_dwordx4 v[160:163], v2, s[28:31], s79 offen sc1\n"
    "s_nop 10\n"
    "buffer_load_dwordx4 v[164:167], v2, s[28:31], s79 offen offset:1024 sc1\n"
    "s_waitcnt vmcnt(9)\n"
    "v_mfma_f32_32x32x16_bf16 v[64:79], a[176:179], v[128:131], 0\n"
    "v_mfma_f32_32x32x16_bf16 v[80:95], a[180:183], v[128:131], 0\n"
    "buffer_load_dwordx4 v[168:171], v2, s[28:31], s79 offen offset:2048 sc1\n"
    "s_waitcnt vmcnt(9)\n"
    "v_mfma_f32_32x32x16_bf16 v[64:79], a[184:187], v[132:135], v[64:79]\n"
    "v_mfma_f32_32x32x16_bf16 v[80:95], a[188:191], v[132:135], v[80:95]\n"
    "buffer_load_dwordx4 v[172:175], v2, s[28:31], s79 offen offset:3072 sc1\n"
    "s_waitcnt vmcnt(9)\n"
    "v_mfma_f32_32x32x16_bf16 v[64:79], a[192:195], v[136:139], v[64:79]\n"
    "v_mfma_f32_32x32x16_bf16 v[80:95], a[196:199], v[136:139], v[80:95]\n"
    "s_xor_b32 s79, s79, 0x1000\n"
    "buffer_load_dwordx4 v[176:179], v2, s[28:31], s79 offen sc1\n"
    "s_waitcnt vmcnt(9)\n"
    "v_mfma_f32_32x32x16_bf16 v[64:79], a[200:203], v[140:143], v[64:79]\n"
    "v_mfma_f32_32x32x16_bf16 v[80:95], a[204:207], v[140:143], v[80:95]\n"
    "buffer_load_dwordx4 v[180:183], v2, s[28:31], s79 offen offset:1024 sc1\n"
    "s_waitcnt vmcnt(9)\n"
    "v_mfma_f32_32x32x16_bf16 v[64:79], a[208:211], v[144:147], v[64:79]\n"
    "v_mfma_f32_32x32x16_bf16 v[80:95], a[212:215], v[144:147], v[80:95]\n"
    "buffer_load_dwordx4 v[184:187], v2, s[28:31], s79 offen offset:2048 sc1\n"
    "buffer_load_dwordx4 v[188:191], v2, s[28:31], s79 offen offset:3072 sc1\n"
    "s_waitcnt vmcnt(10)\n"
    "v_mfma_f32_32x32x16_bf16 v[64:79], a[216:219], v[148:151], v[64:79]\n"
    "v_mfma_f32_32x32x16_bf16 v[80:95], a[220:223], v[148:151], v[80:95]\n"
    "buffer_load_dwordx4 a[0:3], v2, s[28:31], s76 offen sc1\n"
    "buffer_load_dwordx4 a[4:7], v2, s[28:31], s76 offen offset:1024 sc1\n"
    "s_waitcnt vmcnt(11)\n"
    "v_mfma_f32_32x32x16_bf16 v[64:79], a[224:227], v[152:155], v[64:79]\n"
    "v_mfma_f32_32x32x16_bf16 v[80:95], a[228:231], v[152:155], v[80:95]\n"
    "buffer_load_dwordx4 a[8:11], v2, s[28:31], s76 offen offset:2048 sc1\n"
    "s_waitcnt vmcnt(11)\n"
    "v_mfma_f32_32x32x16_bf16 v[64:79], a[232:235], v[156:159], v[64:79]\n"
    "v_mfma_f32_32x32x16_bf16 v[80:95], a[236:239], v[156:159], v[80:95]\n"
    "buffer_load_dwordx4 a[12:15], v2, s[28:31], s76 offen offset:3072 sc1\n"
    "s_waitcnt vmcnt(11)\n"
    "v_mfma_f32_32x32x16_bf16 v[96:111], a[176:179], v[160:163], 0\n"
    "v_mfma_f32_32x32x16_bf16 v[112:127], a[180:183], v[160:163], 0\n"
    "s_xor_b32 s76, s76, 0x1000\n"
    "buffer_load_dwordx4 a[16:19], v2, s[28:31], s76 offen sc1\n"
    "s_waitcnt vmcnt(11)\n"
    "v_mfma_f32_32x32x16_bf16 v[96:111], a[184:187], v[164:167], v[96:111]\n"
    "s_nop 2\n"
    "ds_write_b128 v16, v[64:67]\n"
    "v_mfma_f32_32x32x16_bf16 v[112:127], a[188:191], v[164:167], v[112:127]\n"
    "ds_write_b128 v16, v[80:83] offset:1024\n"
    "buffer_load_dwordx4 a[20:23], v2, s[28:31], s76 offen offset:1024 sc1\n"
    "s_waitcnt vmcnt(11)\n"
    "v_mfma_f32_32x32x16_bf16 v[96:111], a[192:195], v[168:171], v[96:111]\n"
    "ds_write_b128 v16, v[68:71] offset:2048\n"
    "v_mfma_f32_32x32x16_bf16 v[112:127], a[196:199], v[168:171], v[112:127]\n"
    "ds_write_b128 v16, v[84:87] offset:3072\n"
    "buffer_load_dwordx4 a[24:27], v2, s[28:31], s76 offen offset:2048 sc1\n"
    "s_waitcnt vmcnt(11)\n"
    "v_mfma_f32_32x32x16_bf16 v[96:111], a[200:203], v[172:175], v[96:111]\n"
    "ds_write_b128 v17, v[72:75]\n"
    "v_mfma_f32_32x32x16_bf16 v[112:127], a[204:207], v[172:175], v[112:127]\n"
    "ds_write_b128 v17, v[88:91] offset:1024\n"
    "buffer_load_dwordx4 a[28:31], v2, s[28:31], s76 offen offset:3072 sc1\n"
    "s_waitcnt vmcnt(11)\n"
    "v_mfma_f32_32x32x16_bf16 v[96:111], a[208:211], v[176:179], v[96:111]\n"
    "ds_write_b128 v17, v[76:79] offset:2048\n"
    "v_mfma_f32_32x32x16_bf16 v[112:127], a[212:215], v[176:179], v[112:127]\n"
    "ds_write_b128 v17, v[92:95] offset:3072\n"
    "s_xor_b32 s76, s76, 0x3000\n"
    "buffer_load_dwordx4 a[32:35], v2, s[28:31], s76 offen sc1\n"
    "s_waitcnt vmcnt(11)\n"
    "v_mfma_f32_32x32x16_bf16 v[96:111], a[216:219], v[180:183], v[96:111]\n"
    "v_mfma_f32_32x32x16_bf16 v[112:127], a[220:223], v[180:183], v[112:127]\n"
    "s_waitcnt lgkmcnt(0)\n"
    "s_barrier\n"
    "ds_read_b128 v[64:67], v18 offset:4096\n"
    "ds_read_b128 v[68:71], v18 offset:5120\n"
    "ds_read_b128 v[80:83], v18 offset:8192\n"
    "ds_read_b128 v[84:87], v18 offset:9216\n"
    "buffer_load_dwordx4 a[36:39], v2, s[28:31], s76 offen offset:1024 sc1\n"
    "s_waitcnt vmcnt(11)\n"
    "v_mfma_f32_32x32x16_bf16 v[96:111], a[224:227], v[184:187], v[96:111]\n"
    "ds_read_b128 v[72:75], v18 offset:6144\n"
    "ds_read_b128 v[76:79], v18 offset:7168\n"
    "ds_read_b128 v[88:91], v18 offset:10240\n"
    "ds_read_b128 v[92:95], v18 offset:11264\n"
    "v_mfma_f32_32x32x16_bf16 v[112:127], a[228:231], v[184:187], v[112:127]\n"
    "buffer_load_dwordx4 a[40:43], v2, s[28:31], s76 offen offset:2048 sc1\n"
    "s_waitcnt vmcnt(11)\n"
    "v_mfma_f32_32x32x16_bf16 v[96:111], a[232:235], v[188:191], v[96:111]\n"
    "v_mfma_f32_32x32x16_bf16 v[112:127], a[236:239], v[188:191], v[112:127]\n"
    "buffer_load_dwordx4 a[44:47], v2, s[28:31], s76 offen offset:3072 sc1\n"
    "s_waitcnt lgkmcnt(5)\n"
    "v_pk_add_f32 v[64:65], v[64:65], v[80:81]\n"
    "v_pk_add_f32 v[66:67], v[66:67], v[82:83]\n"
    "s_waitcnt lgkmcnt(4)\n"
    "v_pk_add_f32 v[68:69], v[68:69], v[84:85]\n"
    "v_pk_add_f32 v[70:71], v[70:71], v[86:87]\n"
    "s_waitcnt lgkmcnt(1)\n"
    "v_pk_add_f32 v[72:73], v[72:73], v[88:89]\n"
    "v_pk_add_f32 v[74:75], v[74:75], v[90:91]\n"
    "s_waitcnt lgkmcnt(0)\n"
    "v_pk_add_f32 v[76:77], v[76:77], v[92:93]\n"
    "v_pk_add_f32 v[78:79], v[78:79], v[94:95]\n"
    "ds_write_b128 v15, v[104:107]\n"
    "ds_write_b128 v15, v[108:111] offset:2048\n"
    "ds_write_b128 v15, v[120:123] offset:1024\n"
    "ds_write_b128 v15, v[124:127] offset:3072\n"
    "v_pk_add_f32 v[96:97], v[96:97], v[64:65]\n"
    "v_pk_add_f32 v[98:99], v[98:99], v[66:67]\n"
    "v_pk_add_f32 v[112:113], v[112:113], v[68:69]\n"
    "v_pk_add_f32 v[114:115], v[114:115], v[70:71]\n"
    "v_pk_add_f32 v[100:101], v[100:101], v[72:73]\n"
    "v_pk_add_f32 v[102:103], v[102:103], v[74:75]\n"
    "v_pk_add_f32 v[116:117], v[116:117], v[76:77]\n"
    "v_pk_add_f32 v[118:119], v[118:119], v[78:79]\n"
    "s_waitcnt lgkmcnt(0)\n"
    "s_barrier\n"
    "ds_read_b128 v[128:131], v18\n"
    "ds_read_b128 v[132:135], v18 offset:1024\n"
    "ds_read_b128 v[136:139], v18 offset:2048\n"
    "ds_read_b128 v[140:143], v18 offset:3072\n"
    "s_waitcnt lgkmcnt(3)\n"
    "v_pk_add_f32 v[96:97], v[96:97], v[128:129]\n"
    "v_pk_add_f32 v[98:99], v[98:99], v[130:131]\n"
    "v_pk_fma_f32 v[32:33], v[192:193], v[96:97], v[32:33]\n"
    "v_pk_fma_f32 v[34:35], v[194:195], v[98:99], v[34:35]\n"
    "v_pk_mul_f32 v[176:177], v[32:33], v[208:209]\n"
    "v_pk_mul_f32 v[178:179], v[34:35], v[210:211]\n"
    "s_waitcnt lgkmcnt(2)\n"
    "v_pk_add_f32 v[112:113], v[112:113], v[132:133]\n"
    "v_pk_add_f32 v[114:115], v[114:115], v[134:135]\n"
    "v_pk_fma_f32 v[36:37], v[196:197], v[112:113], v[36:37]\n"
    "v_pk_fma_f32 v[38:39], v[198:199], v[114:115], v[38:39]\n"
    "v_pk_mul_f32 v[180:181], v[36:37], v[212:213]\n"
    "v_pk_mul_f32 v[182:183], v[38:39], v[214:215]\n"
    "v_cvt_pk_bf16_f32 v184, v176, v180\n"
    "v_cvt_pk_bf16_f32 v185, v177, v181\n"
    "v_cvt_pk_bf16_f32 v186, v178, v182\n"
    "v_cvt_pk_bf16_f32 v187, v179, v183\n"
    "buffer_store_dwordx4 v[184:187], v2, s[28:31], s80 offen sc1\n"
    "s_waitcnt lgkmcnt(1)\n"
    "v_pk_add_f32 v[100:101], v[100:101], v[136:137]\n"
    "v_pk_add_f32 v[102:103], v[102:103], v[138:139]\n"
    "v_pk_fma_f32 v[40:41], v[200:201], v[100:101], v[40:41]\n"
    "v_pk_fma_f32 v[42:43], v[202:203], v[102:103], v[42:43]\n"
    "v_pk_mul_f32 v[176:177], v[40:41], v[216:217]\n"
    "v_pk_mul_f32 v[178:179], v[42:43], v[218:219]\n"
    "s_waitcnt lgkmcnt(0)\n"
    "v_pk_add_f32 v[116:117], v[116:117], v[140:141]\n"
    "v_pk_add_f32 v[118:119], v[118:119], v[142:143]\n"
    "v_pk_fma_f32 v[44:45], v[204:205], v[116:117], v[44:45]\n"
    "v_pk_fma_f32 v[46:47], v[206:207], v[118:119], v[46:47]\n"
    "v_pk_mul_f32 v[180:181], v[44:45], v[220:221]\n"
    "v_pk_mul_f32 v[182:183], v[46:47], v[222:223]\n"
    "v_cvt_pk_bf16_f32 v188, v176, v180\n"
    "v_cvt_pk_bf16_f32 v189, v177, v181\n"
    "v_cvt_pk_bf16_f32 v190, v178, v182\n"
    "v_cvt_pk_bf16_f32 v191, v179, v183\n"
    "buffer_store_dwordx4 v[188:191], v2, s[28:31], s80 offen offset:1024 sc1\n"
    "v_pk_add_f32 v[176:177], v[32:33], v[34:35]\n"
    "v_pk_mul_f32 v[178:179], v[32:33], v[32:33]\n"
    "v_pk_fma_f32 v[178:179], v[34:35], v[34:35], v[178:179]\n"
    "v_pk_add_f32 v[176:177], v[176:177], v[36:37]\n"
    "v_pk_fma_f32 v[178:179], v[36:37], v[36:37], v[178:179]\n"
    "v_pk_add_f32 v[176:177], v[176:177], v[38:39]\n"
    "v_pk_fma_f32 v[178:179], v[38:39], v[38:39], v[178:179]\n"
    "v_pk_add_f32 v[176:177], v[176:177], v[40:41]\n"
    "v_pk_fma_f32 v[178:179], v[40:41], v[40:41], v[178:179]\n"
    "v_pk_add_f32 v[176:177], v[176:177], v[42:43]\n"
    "v_pk_fma_f32 v[178:179], v[42:43], v[42:43], v[178:179]\n"
    "v_pk_add_f32 v[176:177], v[176:177], v[44:45]\n"
    "v_pk_fma_f32 v[178:179], v[44:45], v[44:45], v[178:179]\n"
    "v_pk_add_f32 v[176:177], v[176:177], v[46:47]\n"
    "v_pk_fma_f32 v[178:179], v[46:47], v[46:47], v[178:179]\n"
    "v_add_f32 v176, v176, v177\n"
    "v_add_f32 v177, v178, v179\n"
    "v_mov_b32 v178, v176\n"
    "v_mov_b32 v179, v177\n"
    "s_lshl_b32 s5, s15, 13\n"
    "v_permlane32_swap_b32 v176, v178\n"
    "v_permlane32_swap_b32 v177, v179\n"
    "v_pk_add_f32 v[176:177], v[176:177], v[178:179]\n"
    "v_mov_b32 v178, v176\n"
    "v_mov_b32 v179, v177\n"
    "v_add_u32 v28, s5, v8\n"
    "v_permlane16_swap_b32 v176, v178\n"
    "v_permlane16_swap_b32 v177, v179\n"
    "s_mov_b64 exec, 0xffff\n"
    "buffer_store_dwordx4 v[176:179], v2, s[28:31], s81 offen sc1\n"
    "s_mov_b64 exec, -1\n"
    "v_add_u32 v29, s5, v9\n"
    "ds_read_b128 v[128:131], v25\n"
    "ds_read_b64_tr_b16 v[212:213], v28\n"
    "ds_read_b64_tr_b16 v[214:215], v28 offset:256\n"
    "ds_read_b64_tr_b16 v[216:217], v28 offset:32768\n"
    "ds_read_b64_tr_b16 v[218:219], v28 offset:33024\n"
    "ds_read_b64_tr_b16 v[220:221], v29\n"
    "ds_read_b64_tr_b16 v[222:223], v29 offset:256\n"
    "ds_read_b128 v[132:135], v25 offset:32\n"
    "ds_read_b128 v[136:139], v25 offset:64\n"
    "ds_read_b128 v[140:143], v25 offset:96\n"
    "ds_read_b128 v[144:147], v25 offset:128\n"
    "ds_read_b128 v[148:151], v25 offset:160\n"
    "ds_read_b128 v[152:155], v25 offset:192\n"
    "ds_read_b128 v[156:159], v25 offset:224\n"
    "s_waitcnt lgkmcnt(11)\n"
    "v_mfma_f32_32x32x16_bf16 v[64:79], v[128:131], v[212:215], 0\n"
    "ds_read_b64_tr_b16 v[224:225], v28 offset:1024\n"
    "ds_read_b64_tr_b16 v[226:227], v28 offset:1280\n"
    "s_xor_b32 s76, s76, 0x1000\n"
    "buffer_load_dwordx4 a[48:51], v2, s[28:31], s76 offen sc1\n"
    "s_waitcnt lgkmcnt(11)\n"
    "v_mfma_f32_32x32x16_bf16 v[80:95], v[128:131], v[216:219], 0\n"
    "ds_read_b64_tr_b16 v[228:229], v28 offset:33792\n"
    "ds_read_b64_tr_b16 v[230:231], v28 offset:34048\n"
    "s_waitcnt lgkmcnt(11)\n"
    "v_mfma_f32_32x32x16_bf16 v[96:111], v[128:131], v[220:223], 0\n"
    "ds_read_b64_tr_b16 v[232:233], v29 offset:1024\n"
    "ds_read_b64_tr_b16 v[234:235], v29 offset:1280\n"
    "buffer_load_dwordx4 a[52:55], v2, s[28:31], s76 offen offset:1024 sc1\n"
    "s_waitcnt lgkmcnt(4)\n"
    "v_mfma_f32_32x32x16_bf16 v[64:79], v[132:135], v[224:227], v[64:79]\n"
    "ds_read_b64_tr_b16 v[212:213], v28 offset:2048\n"
    "ds_read_b64_tr_b16 v[214:215], v28 offset:2304\n"
    "s_waitcnt lgkmcnt(4)\n"
    "v_mfma_f32_32x32x16_bf16 v[80:95], v[132:135], v[228:231], v[80:95]\n"
    "ds_read_b64_tr_b16 v[216:217], v28 offset:34816\n"
    "ds_read_b64_tr_b16 v[218:219], v28 offset:35072\n"
    "s_waitcnt lgkmcnt(4)\n"
    "v_mfma_f32_32x32x16_bf16 v[96:111], v[132:135], v[232:235], v[96:111]\n"
    "ds_read_b64_tr_b16 v[220:221], v29 offset:2048\n"
    "ds_read_b64_tr_b16 v[222:223], v29 offset:2304\n"
    "buffer_load_dwordx4 a[56:59], v2, s[28:31], s76 offen offset:2048 sc1\n"
    "s_waitcnt lgkmcnt(4)\n"
    "v_mfma_f32_32x32x16_bf16 v[64:79], v[136:139], v[212:215], v[64:79]\n"
    "ds_read_b64_tr_b16 v[224:225], v28 offset:3072\n"
    "ds_read_b64_tr_b16 v[226:227], v28 offset:3328\n"
    "s_waitcnt lgkmcnt(4)\n"
    "v_mfma_f32_32x32x16_bf16 v[80:95], v[136:139], v[216:219], v[80:95]\n"
    "ds_read_b64_tr_b16 v[228:229], v28 offset:35840\n"
    "ds_read_b64_tr_b16 v[230:231], v28 offset:36096\n"
    "buffer_load_dwordx4 a[60:63], v2, s[28:31], s76 offen offset:3072 sc1\n"
    "s_waitcnt lgkmcnt(4)\n"
    "v_mfma_f32_32x32x16_bf16 v[96:111], v[136:139], v[220:223], v[96:111]\n"
    "ds_read_b64_tr_b16 v[232:233], v29 offset:3072\n"
    "ds_read_b64_tr_b16 v[234:235], v29 offset:3328\n"
    "s_waitcnt lgkmcnt(4)\n"
    "v_mfma_f32_32x32x16_bf16 v[64:79], v[140:143], v[224:227], v[64:79]\n"
    "ds_read_b64_tr_b16 v[212:213], v28 offset:4096\n"
    "ds_read_b64_tr_b16 v[214:215], v28 offset:4352\n"
    "s_waitcnt lgkmcnt(4)\n"
    "v_mfma_f32_32x32x16_bf16 v[80:95], v[140:143], v[228:231], v[80:95]\n"
    "ds_read_b64_tr_b16 v[216:217], v28 offset:36864\n"
    "ds_read_b64_tr_b16 v[218:219], v28 offset:37120\n"
    "s_xor_b32 s76, s76, 0x7000\n"
    "buffer_load_dwordx4 a[64:67], v2, s[28:31], s76 offen sc1\n"
    "s_waitcnt vmcnt(5)\n"
    "s_barrier\n"
    "s_cmp_lg_u32 s15, 1\n"
    "s_cbranch_scc1 .Ls12\n"
    "s_mov_b64 exec, 0xffff\n"
    "buffer_store_dwordx2 v[6:7], v19, s[28:31], s62 offen sc1\n"
    "s_mov_b64 exec, -1\n"
    ".Ls12:\n"
    "s_add_u32 s62, s62, 0x20000\n"
    "s_waitcnt lgkmcnt(4)\n"
    "v_mfma_f32_32x32x16_bf16 v[96:111], v[140:143], v[232:235], v[96:111]\n"
    "ds_read_b64_tr_b16 v[220:221], v29 offset:4096\n"
    "ds_read_b64_tr_b16 v[222:223], v29 offset:4352\n"
    "s_waitcnt lgkmcnt(4)\n"
    "v_mfma_f32_32x32x16_bf16 v[64:79], v[144:147], v[212:215], v[64:79]\n"
    "ds_read_b64_tr_b16 v[224:225], v28 offset:5120\n"
    "ds_read_b64_tr_b16 v[226:227], v28 offset:5376\n"
    "buffer_load_dwordx4 a[68:71], v2, s[28:31], s76 offen offset:1024 sc1\n"
    "s_waitcnt lgkmcnt(4)\n"
    "v_mfma_f32_32x32x16_bf16 v[80:95], v[144:147], v[216:219], v[80:95]\n"
    "ds_read_b64_tr_b16 v[228:229], v28 offset:37888\n"
    "ds_read_b64_tr_b16 v[230:231], v28 offset:38144\n"
    "s_waitcnt lgkmcnt(4)\n"
    "v_mfma_f32_32x32x16_bf16 v[96:111], v[144:147], v[220:223], v[96:111]\n"
    "ds_read_b64_tr_b16 v[232:233], v29 offset:5120\n"
    "ds_read_b64_tr_b16 v[234:235], v29 offset:5376\n"
    "s_waitcnt lgkmcnt(4)\n"
    "v_mfma_f32_32x32x16_bf16 v[64:79], v[148:151], v[224:227], v[64:79]\n"
    "ds_read_b64_tr_b16 v[212:213], v28 offset:6144\n"
    "ds_read_b64_tr_b16 v[214:215], v28 offset:6400\n"
    "buffer_load_dwordx4 a[72:75], v2, s[28:31], s76 offen offset:2048 sc1\n"
    "s_waitcnt lgkmcnt(4)\n"
    "v_mfma_f32_32x32x16_bf16 v[80:95], v[148:151], v[228:231], v[80:95]\n"
    "ds_read_b64_tr_b16 v[216:217], v28 offset:38912\n"
    "ds_read_b64_tr_b16 v[218:219], v28 offset:39168\n"
    "s_waitcnt lgkmcnt(4)\n"
    "v_mfma_f32_32x32x16_bf16 v[96:111], v[148:151], v[232:235], v[96:111]\n"
    "ds_read_b64_tr_b16 v[220:221], v29 offset:6144\n"
    "ds_read_b64_tr_b16 v[222:223], v29 offset:6400\n"
    "buffer_load_dwordx4 a[76:79], v2, s[28:31], s76 offen offset:3072 sc1\n"
    "s_waitcnt lgkmcnt(4)\n"
    "v_mfma_f32_32x32x16_bf16 v[64:79], v[152:155], v[212:215], v[64:79]\n"
    "ds_read_b64_tr_b16 v[224:225], v28 offset:7168\n"
    "ds_read_b64_tr_b16 v[226:227], v28 offset:7424\n"
    "s_waitcnt lgkmcnt(4)\n"
    "v_mfma_f32_32x32x16_bf16 v[80:95], v[152:155], v[216:219], v[80:95]\n"
    "ds_read_b64_tr_b16 v[228:229], v28 offset:39936\n"
    "ds_read_b64_tr_b16 v[230:231], v28 offset:40192\n"
    "s_waitcnt lgkmcnt(4)\n"
    "v_mfma_f32_32x32x16_bf16 v[96:111], v[152:155], v[220:223], v[96:111]\n"
    "ds_read_b64_tr_b16 v[232:233], v29 offset:7168\n"
    "ds_read_b64_tr_b16 v[234:235], v29 offset:7424\n"
    "s_xor_b32 s76, s76, 0x1000\n"
    "buffer_load_dwordx4 a[80:83], v2, s[28:31], s76 offen sc1\n"
    "s_waitcnt lgkmcnt(4)\n"
    "v_mfma_f32_32x32x16_bf16 v[64:79], v[156:159], v[224:227], v[64:79]\n"
    "s_waitcnt lgkmcnt(2)\n"
    "v_mfma_f32_32x32x16_bf16 v[80:95], v[156:159], v[228:231], v[80:95]\n"
    "buffer_load_dwordx4 a[84:87], v2, s[28:31], s76 offen offset:1024 sc1\n"
    "s_waitcnt lgkmcnt(0)\n"
    "v_mfma_f32_32x32x16_bf16 v[96:111], v[156:159], v[232:235], v[96:111]\n"
    "s_lshl_b32 s4, s15, 7\n"
    "s_add_u32 s4, s4, 0x500\n"
    "v_lshl_add_u32 v28, v4, 2, s4\n"
    "s_and_b32 s5, s59, 1\n"
    "s_lshl_b32 s4, s5, 8\n"
    "s_lshl_b32 s7, s15, 6\n"
    "s_add_u32 s4, s4, s7\n"
    "s_add_u32 s4, s4, 0x500\n"
    "v_lshl_add_u32 v29, v5, 4, s4\n"
    "ds_write_b64 v26, v[64:65] offset:12288\n"
    "ds_write_b64 v26, v[80:81] offset:13312\n"
    "s_nop 0\n"
    "ds_write_b64 v26, v[96:97] offset:14336\n"
    "ds_read_b64_tr_b16 a[176:177], v8\n"
    "ds_read_b64_tr_b16 a[178:179], v8 offset:256\n"
    "ds_read_b64_tr_b16 a[180:181], v8 offset:32768\n"
    "ds_read_b64_tr_b16 a[182:183], v8 offset:33024\n"
    "ds_read_b64_tr_b16 a[184:185], v9\n"
    "ds_read_b64_tr_b16 a[186:187], v9 offset:256\n"
    "ds_read_b64_tr_b16 a[188:189], v8 offset:1024\n"
    "ds_read_b64_tr_b16 a[190:191], v8 offset:1280\n"
    "s_waitcnt lgkmcnt(0)\n"
    "s_cmp_lg_u32 s15, 0\n"
    "s_cbranch_scc1 .Lr13\n"
    "v_not_b32 v31, v7\n"
    "s_mov_b64 exec, 0xffff\n"
    "buffer_load_dwordx2 v[30:31], v3, s[28:31], s64 offen sc1\n"
    "s_mov_b64 exec, -1\n"
    ".Lr13:\n"
    "buffer_load_dwordx4 a[88:91], v2, s[28:31], s76 offen offset:2048 sc1\n"
    "buffer_load_dwordx4 a[92:95], v2, s[28:31], s76 offen offset:3072 sc1\n"
    "s_xor_b32 s76, s76, 0x3000\n"
    "buffer_load_dwordx4 a[96:99], v2, s[28:31], s76 offen sc1\n"
    "buffer_load_dwordx4 a[100:103], v2, s[28:31], s76 offen offset:1024 sc1\n"
    "buffer_load_dwordx4 a[104:107], v2, s[28:31], s76 offen offset:2048 sc1\n"
    "buffer_load_dwordx4 a[108:111], v2, s[28:31], s76 offen offset:3072 sc1\n"
    "s_cmp_lg_u32 s15, 0\n"
    "s_cbranch_scc1 .Lr14\n"
    "s_mov_b64 exec, 0xffff\n"
    "s_waitcnt vmcnt(6)\n"
    "v_cmp_ne_u64 vcc, s[12:13], v[30:31]\n"
    "s_cbranch_vccz .Lq14\n"
    ".Lp14:\n"
    "buffer_load_dwordx2 v[30:31], v3, s[28:31], s64 offen sc1\n"
    "s_sub_i32 s14, s14, 1\n"
    "s_cmp_lt_i32 s14, 0\n"
    "s_waitcnt vmcnt(0)\n"
    "s_cbranch_scc1 .Lq14\n"
    "v_cmp_ne_u64 vcc, s[12:13], v[30:31]\n"
    "s_cbranch_vccnz .Lp14\n"
    ".Lq14:\n"
    "s_mov_b64 exec, -1\n"
    ".Lr14:\n"
    "s_barrier\n"
    "s_add_u32 s64, s64, 0x20000\n"
    "buffer_load_dwordx4 v[128:131], v2, s[28:31], s82 offen sc1\n"
    "ds_read_b128 v[64:67], v14 offset:12288\n"
    "ds_read_b128 v[68:71], v14 offset:12304\n"
    "ds_read_b128 v[72:75], v14 offset:13312\n"
    "ds_read_b128 v[76:79], v14 offset:13328\n"
    "ds_read_b128 v[80:83], v14 offset:14336\n"
    "ds_read_b128 v[84:87], v14 offset:14352\n"
    "s_nop 7\n"
    "buffer_load_dwordx4 v[132:135], v2, s[28:31], s82 offen offset:1024 sc1\n"
    "s_mov_b32 vcc_lo, -1\n"
    "s_mov_b32 vcc_hi, 0\n"
    "s_waitcnt lgkmcnt(5)\n"
    "v_pk_add_f32 v[64:65], v[64:65], v[66:67]\n"
    "s_waitcnt lgkmcnt(4)\n"
    "v_pk_add_f32 v[68:69], v[68:69], v[70:71]\n"
    "v_pk_add_f32 v[64:65], v[64:65], v[68:69]\n"
    "v_cvt_pk_bf16_f32 v66, v64, v65\n"
    "v_lshlrev_b32 v68, 16, v66\n"
    "buffer_load_dwordx4 v[136:139], v2, s[28:31], s82 offen offset:2048 sc1\n"
    "v_and_b32 v69, s75, v66\n"
    "v_sub_f32 v70, v64, v68\n"
    "v_sub_f32 v71, v65, v69\n"
    "v_cvt_pk_bf16_f32 v64, v68, v70\n"
    "v_cvt_pk_bf16_f32 v65, v71, v69\n"
    "v_cndmask_b32 v48, 0, v64, vcc\n"
    "v_cndmask_b32 v49, 0, v66, vcc\n"
    "buffer_load_dwordx4 v[140:143], v2, s[28:31], s82 offen offset:3072 sc1\n"
    "v_cndmask_b32 v50, 0, v65, vcc\n"
    "v_mov_b32 v51, 0\n"
    "s_waitcnt lgkmcnt(3)\n"
    "v_pk_add_f32 v[72:73], v[72:73], v[74:75]\n"
    "s_waitcnt lgkmcnt(2)\n"
    "v_pk_add_f32 v[76:77], v[76:77], v[78:79]\n"
    "v_pk_add_f32 v[72:73], v[72:73], v[76:77]\n"
    "v_cvt_pk_bf16_f32 v74, v72, v73\n"
    "v_lshlrev_b32 v76, 16, v74\n"
    "s_add_u32 s82, s82, 0x1000\n"
    "buffer_load_dwordx4 v[144:147], v2, s[28:31], s82 offen sc1\n"
    "v_and_b32 v77, s75, v74\n"
    "v_sub_f32 v78, v72, v76\n"
    "v_sub_f32 v79, v73, v77\n"
    "v_cvt_pk_bf16_f32 v72, v76, v78\n"
    "v_cvt_pk_bf16_f32 v73, v79, v77\n"
    "v_cndmask_b32 v52, 0, v72, vcc\n"
    "buffer_load_dwordx4 v[148:151], v2, s[28:31], s82 offen offset:1024 sc1\n"
    "v_cndmask_b32 v53, 0, v74, vcc\n"
    "v_cndmask_b32 v54, 0, v73, vcc\n"
    "v_mov_b32 v55, 0\n"
    "s_waitcnt lgkmcnt(1)\n"
    "v_pk_add_f32 v[80:81], v[80:81], v[82:83]\n"
    "s_waitcnt lgkmcnt(0)\n"
    "v_pk_add_f32 v[84:85], v[84:85], v[86:87]\n"
    "v_pk_add_f32 v[80:81], v[80:81], v[84:85]\n"
    "v_cvt_pk_bf16_f32 v82, v80, v81\n"
    "buffer_load_dwordx4 v[152:155], v2, s[28:31], s82 offen offset:2048 sc1\n"
    "v_lshlrev_b32 v84, 16, v82\n"
    "v_and_b32 v85, s75, v82\n"
    "v_sub_f32 v86, v80, v84\n"
    "v_sub_f32 v87, v81, v85\n"
    "v_cvt_pk_bf16_f32 v80, v84, v86\n"
    "v_cvt_pk_bf16_f32 v81, v87, v85\n"
    "v_cndmask_b32 v56, 0, v80, vcc\n"
    "buffer_load_dwordx4 v[156:159], v2, s[28:31], s82 offen offset:3072 sc1\n"
    "v_cndmask_b32 v57, 0, v82, vcc\n"
    "v_cndmask_b32 v58, 0, v81, vcc\n"
    "v_mov_b32 v59, 0\n"
    "v_mul_u32_u24 v10, 0x90, v4\n"
    "v_lshl_add_u32 v10, v5, 4, v10\n"
    "s_mul_i32 s4, s2, 0x2400\n"
    "s_lshl_b32 s7, s3, 6\n"
    "s_add_u32 s4, s4, s7\n"
    "s_add_u32 s4, s4, 0x1c000\n"
    "s_add_u32 s82, s82, 0x1000\n"
    "buffer_load_dwordx4 v[160:163], v2, s[28:31], s82 offen sc1\n"
    "v_add_u32 v10, s4, v10\n"
    "ds_write_b128 v10, v[32:35]\n"
    "buffer_load_dwordx4 v[164:167], v2, s[28:31], s82 offen offset:1024 sc1\n"
    "ds_write_b128 v10, v[36:39] offset:4608\n"
    "ds_write_b128 v10, v[40:43] offset:32\n"
    "buffer_load_dwordx4 v[168:171], v2, s[28:31], s82 offen offset:2048 sc1\n"
    "ds_write_b128 v10, v[44:47] offset:4640\n"
    "v_mul_u32_u24 v22, 0x240, v5\n"
    "v_lshl_add_u32 v22, v4, 2, v22\n"
    "s_mul_i32 s4, s2, 0x2400\n"
    "s_mul_i32 s7, s3, 0x900\n"
    "s_add_u32 s4, s4, s7\n"
    "s_add_u32 s4, s4, 0x1c000\n"
    "v_add_u32 v22, s4, v22\n"
    "v_lshlrev_b32 v11, 12, v5\n"
    "v_lshl_or_b32 v11, v4, 2, v11\n"
    "s_lshl_b32 s86, s59, 16\n"
    "s_lshl_b32 s7, s15, 14\n"
    "s_add_u32 s86, s86, s7\n"
    "s_lshl_b32 s7, s60, 7\n"
    "s_add_u32 s86, s86, s7\n"
    "s_lshl_b32 s4, s60, 7\n"
    "s_add_u32 s4, s4, 0x2c00\n"
    "v_lshl_add_u32 v24, v4, 2, s4\n"
    "s_waitcnt vmcnt(10)\n"
    "v_mfma_f32_32x32x16_bf16 v[64:79], a[176:179], v[128:131], 0\n"
    "ds_read_b64_tr_b16 a[192:193], v8 offset:33792\n"
    "ds_read_b64_tr_b16 a[194:195], v8 offset:34048\n"
    "v_mfma_f32_32x32x16_bf16 v[80:95], a[180:183], v[128:131], 0\n"
    "ds_read_b64_tr_b16 a[196:197], v9 offset:1024\n"
    "ds_read_b64_tr_b16 a[198:199], v9 offset:1280\n"
    "buffer_load_dwordx4 v[172:175], v2, s[28:31], s82 offen offset:3072 sc1\n"
    "v_mfma_f32_32x32x16_bf16 v[96:111], a[184:187], v[128:131], 0\n"
    "ds_read_b64_tr_b16 a[200:201], v8 offset:2048\n"
    "ds_read_b64_tr_b16 a[202:203], v8 offset:2304\n"
    "s_waitcnt vmcnt(10)\n"
    "v_mfma_f32_32x32x16_bf16 v[64:79], a[188:191], v[132:135], v[64:79]\n"
    "ds_read_b64_tr_b16 a[204:205], v8 offset:34816\n"
    "ds_read_b64_tr_b16 a[206:207], v8 offset:35072\n"
    "buffer_load_dwordx4 v[112:115], v2, s[28:31], s83 offen sc1\n"
    "s_waitcnt lgkmcnt(6)\n"
    "v_mfma_f32_32x32x16_bf16 v[80:95], a[192:195], v[132:135], v[80:95]\n"
    "ds_read_b64_tr_b16 a[176:177], v9 offset:2048\n"
    "ds_read_b64_tr_b16 a[178:179], v9 offset:2304\n"
    "s_waitcnt lgkmcnt(6)\n"
    "v_mfma_f32_32x32x16_bf16 v[96:111], a[196:199], v[132:135], v[96:111]\n"
    "ds_read_b64_tr_b16 a[180:181], v8 offset:3072\n"
    "ds_read_b64_tr_b16 a[182:183], v8 offset:3328\n"
    "buffer_load_dwordx4 v[116:119], v2, s[28:31], s83 offen offset:1024 sc1\n"
    "s_waitcnt vmcnt(11) lgkmcnt(6)\n"
    "v_mfma_f32_32x32x16_bf16 v[64:79], a[200:203], v[136:139], v[64:79]\n"
    "ds_read_b64_tr_b16 a[184:185], v8 offset:35840\n"
    "ds_read_b64_tr_b16 a[186:187], v8 offset:36096\n"
    "s_waitcnt lgkmcnt(6)\n"
    "v_mfma_f32_32x32x16_bf16 v[80:95], a[204:207], v[136:139], v[80:95]\n"
    "ds_read_b64_tr_b16 a[188:189], v9 offset:3072\n"
    "ds_read_b64_tr_b16 a[190:191], v9 offset:3328\n"
    "buffer_load_dwordx4 v[120:123], v2, s[28:31], s83 offen offset:2048 sc1\n"
    "s_waitcnt lgkmcnt(6)\n"
    "v_mfma_f32_32x32x16_bf16 v[96:111], a[176:179], v[136:139], v[96:111]\n"
    "ds_read_b64_tr_b16 a[192:193], v8 offset:4096\n"
    "ds_read_b64_tr_b16 a[194:195], v8 offset:4352\n"
    "s_waitcnt vmcnt(11) lgkmcnt(6)\n"
    "v_mfma_f32_32x32x16_bf16 v[64:79], a[180:183], v[140:143], v[64:79]\n"
    "ds_read_b64_tr_b16 a[196:197], v8 offset:36864\n"
    "ds_read_b64_tr_b16 a[198:199], v8 offset:37120\n"
    "buffer_load_dwordx4 v[124:127], v2, s[28:31], s83 offen offset:3072 sc1\n"
    "s_waitcnt lgkmcnt(6)\n"
    "v_mfma_f32_32x32x16_bf16 v[80:95], a[184:187], v[140:143], v[80:95]\n"
    "ds_read_b64_tr_b16 a[200:201], v9 offset:4096\n"
    "ds_read_b64_tr_b16 a[202:203], v9 offset:4352\n"
    "s_waitcnt lgkmcnt(6)\n"
    "v_mfma_f32_32x32x16_bf16 v[96:111], a[188:191], v[140:143], v[96:111]\n"
    "ds_read_b64_tr_b16 a[204:205], v8 offset:5120\n"
    "ds_read_b64_tr_b16 a[206:207], v8 offset:5376\n"
    "s_add_u32 s82, s82, 0x1000\n"
    "buffer_load_dwordx4 v[176:179], v2, s[28:31], s82 offen sc1\n"
    "s_waitcnt vmcnt(12) lgkmcnt(6)\n"
    "v_mfma_f32_32x32x16_bf16 v[64:79], a[192:195], v[144:147], v[64:79]\n"
    "ds_read_b64_tr_b16 a[176:177], v8 offset:37888\n"
    "ds_read_b64_tr_b16 a[178:179], v8 offset:38144\n"
    "s_waitcnt lgkmcnt(6)\n"
    "v_mfma_f32_32x32x16_bf16 v[80:95], a[196:199], v[144:147], v[80:95]\n"
    "ds_read_b64_tr_b16 a[180:181], v9 offset:5120\n"
    "ds_read_b64_tr_b16 a[182:183], v9 offset:5376\n"
    "buffer_load_dwordx4 v[180:183], v2, s[28:31], s82 offen offset:1024 sc1\n"
    "s_waitcnt lgkmcnt(6)\n"
    "v_mfma_f32_32x32x16_bf16 v[96:111], a[200:203], v[144:147], v[96:111]\n"
    "ds_read_b64_tr_b16 a[184:185], v8 offset:6144\n"
    "ds_read_b64_tr_b16 a[186:187], v8 offset:6400\n"
    "s_waitcnt vmcnt(12) lgkmcnt(6)\n"
    "v_mfma_f32_32x32x16_bf16 v[64:79], a[204:207], v[148:151], v[64:79]\n"
    "ds_read_b64_tr_b16 a[188:189], v8 offset:38912\n"
    "ds_read_b64_tr_b16 a[190:191], v8 offset:39168\n"
    "buffer_load_dwordx4 v[184:187], v2, s[28:31], s82 offen offset:2048 sc1\n"
    "s_waitcnt lgkmcnt(6)\n"
    "v_mfma_f32_32x32x16_bf16 v[80:95], a[176:179], v[148:151], v[80:95]\n"
    "ds_read_b64_tr_b16 a[192:193], v8 offset:7168\n"
    "ds_read_b64_tr_b16 a[194:195], v8 offset:7424\n"
    "s_waitcnt lgkmcnt(6)\n"
    "v_mfma_f32_32x32x16_bf16 v[96:111], a[180:183], v[148:151], v[96:111]\n"
    "ds_read_b64_tr_b16 a[196:197], v8 offset:39936\n"
    "ds_read_b64_tr_b16 a[198:199], v8 offset:40192\n"
    "buffer_load_dwordx4 v[188:191], v2, s[28:31], s82 offen offset:3072 sc1\n"
    "s_waitcnt vmcnt(13) lgkmcnt(6)\n"
    "v_mfma_f32_32x32x16_bf16 v[64:79], a[184:187], v[152:155], v[64:79]\n"
    "ds_read_b64_tr_b16 a[200:201], v8 offset:8192\n"
    "ds_read_b64_tr_b16 a[202:203], v8 offset:8448\n"
    "s_waitcnt lgkmcnt(6)\n"
    "v_mfma_f32_32x32x16_bf16 v[80:95], a[188:191], v[152:155], v[80:95]\n"
    "ds_read_b64_tr_b16 a[204:205], v8 offset:40960\n"
    "ds_read_b64_tr_b16 a[206:207], v8 offset:41216\n"
    "s_add_u32 s82, s82, 0x1000\n"
    "buffer_load_dwordx4 v[192:195], v2, s[28:31], s82 offen sc1\n"
    "s_waitcnt vmcnt(13) lgkmcnt(6)\n"
    "v_mfma_f32_32x32x16_bf16 v[64:79], a[192:195], v[156:159], v[64:79]\n"
    "ds_read_b64_tr_b16 a[176:177], v8 offset:9216\n"
    "ds_read_b64_tr_b16 a[178:179], v8 offset:9472\n"
    "s_waitcnt lgkmcnt(6)\n"
    "v_mfma_f32_32x32x16_bf16 v[80:95], a[196:199], v[156:159], v[80:95]\n"
    "ds_read_b64_tr_b16 a[180:181], v8 offset:41984\n"
    "ds_read_b64_tr_b16 a[182:183], v8 offset:42240\n"
    "buffer_load_dwordx4 v[196:199], v2, s[28:31], s82 offen offset:1024 sc1\n"
    "s_waitcnt vmcnt(13) lgkmcnt(6)\n"
    "v_mfma_f32_32x32x16_bf16 v[64:79], a[200:203], v[160:163], v[64:79]\n"
    "ds_read_b64_tr_b16 a[184:185], v8 offset:10240\n"
    "ds_read_b64_tr_b16 a[186:187], v8 offset:10496\n"
    "s_waitcnt lgkmcnt(6)\n"
    "v_mfma_f32_32x32x16_bf16 v[80:95], a[204:207], v[160:163], v[80:95]\n"
    "ds_read_b64_tr_b16 a[188:189], v8 offset:43008\n"
    "ds_read_b64_tr_b16 a[190:191], v8 offset:43264\n"
    "buffer_load_dwordx4 v[200:203], v2, s[28:31], s82 offen offset:2048 sc1\n"
    "s_waitcnt vmcnt(13) lgkmcnt(6)\n"
    "v_mfma_f32_32x32x16_bf16 v[64:79], a[176:179], v[164:167], v[64:79]\n"
    "ds_read_b64_tr_b16 a[192:193], v8 offset:11264\n"
    "ds_read_b64_tr_b16 a[194:195], v8 offset:11520\n"
    "s_waitcnt lgkmcnt(6)\n"
    "v_mfma_f32_32x32x16_bf16 v[80:95], a[180:183], v[164:167], v[80:95]\n"
    "ds_read_b64_tr_b16 a[196:197], v8 offset:44032\n"
    "ds_read_b64_tr_b16 a[198:199], v8 offset:44288\n"
    "buffer_load_dwordx4 v[204:207], v2, s[28:31], s82 offen offset:3072 sc1\n"
    "s_waitcnt vmcnt(13) lgkmcnt(6)\n"
    "v_mfma_f32_32x32x16_bf16 v[64:79], a[184:187], v[168:171], v[64:79]\n"
    "ds_read_b64_tr_b16 a[200:201], v8 offset:12288\n"
    "ds_read_b64_tr_b16 a[202:203], v8 offset:12544\n"
    "s_waitcnt lgkmcnt(6)\n"
    "v_mfma_f32_32x32x16_bf16 v[80:95], a[188:191], v[168:171], v[80:95]\n"
    "ds_read_b64_tr_b16 a[204:205], v8 offset:45056\n"
    "ds_read_b64_tr_b16 a[206:207], v8 offset:45312\n"
    "s_add_u32 s82, s82, 0x1000\n"
    "buffer_load_dwordx4 v[208:211], v2, s[28:31], s82 offen sc1\n"
    "s_waitcnt vmcnt(13) lgkmcnt(6)\n"
    "v_mfma_f32_32x32x16_bf16 v[64:79], a[192:195], v[172:175], v[64:79]\n"
    "ds_read_b64_tr_b16 a[176:177], v8 offset:13312\n"
    "ds_read_b64_tr_b16 a[178:179], v8 offset:13568\n"
    "s_waitcnt lgkmcnt(6)\n"
    "v_mfma_f32_32x32x16_bf16 v[80:95], a[196:199], v[172:175], v[80:95]\n"
    "ds_read_b64_tr_b16 a[180:181], v8 offset:46080\n"
    "ds_read_b64_tr_b16 a[182:183], v8 offset:46336\n"
    "buffer_load_dwordx4 v[212:215], v2, s[28:31], s82 offen offset:1024 sc1\n"
    "s_waitcnt vmcnt(9) lgkmcnt(6)\n"
    "v_mfma_f32_32x32x16_bf16 v[64:79], a[200:203], v[176:179], v[64:79]\n"
    "ds_read_b64_tr_b16 a[184:185], v8 offset:14336\n"
    "ds_read_b64_tr_b16 a[186:187], v8 offset:14592\n"
    "s_waitcnt lgkmcnt(6)\n"
    "v_mfma_f32_32x32x16_bf16 v[80:95], a[204:207], v[176:179], v[80:95]\n"
    "ds_read_b64_tr_b16 a[188:189], v8 offset:47104\n"
    "ds_read_b64_tr_b16 a[190:191], v8 offset:47360\n"
    "buffer_load_dwordx4 v[216:219], v2, s[28:31], s82 offen offset:2048 sc1\n"
    "s_waitcnt vmcnt(9) lgkmcnt(6)\n"
    "v_mfma_f32_32x32x16_bf16 v[64:79], a[176:179], v[180:183], v[64:79]\n"
    "ds_read_b64_tr_b16 a[192:193], v8 offset:15360\n"
    "ds_read_b64_tr_b16 a[194:195], v8 offset:15616\n"
    "v_add_f32 v112, v112, v116\n"
    "v_add_f32 v113, v113, v117\n"
    "v_add_f32 v114, v114, v118\n"
    "s_waitcnt lgkmcnt(6)\n"
    "v_mfma_f32_32x32x16_bf16 v[80:95], a[180:183], v[180:183], v[80:95]\n"
    "ds_read_b64_tr_b16 a[196:197], v8 offset:48128\n"
    "ds_read_b64_tr_b16 a[198:199], v8 offset:48384\n"
    "buffer_load_dwordx4 v[220:223], v2, s[28:31], s82 offen offset:3072 sc1\n"
    "v_add_f32 v115, v115, v119\n"
    "s_waitcnt vmcnt(9) lgkmcnt(6)\n"
    "v_mfma_f32_32x32x16_bf16 v[64:79], a[184:187], v[184:187], v[64:79]\n"
    "ds_read_b64_tr_b16 a[200:201], v8 offset:16384\n"
    "ds_read_b64_tr_b16 a[202:203], v8 offset:16640\n"
    "v_add_f32 v112, v112, v120\n"
    "v_add_f32 v113, v113, v121\n"
    "v_add_f32 v114, v114, v122\n"
    "s_waitcnt lgkmcnt(6)\n"
    "v_mfma_f32_32x32x16_bf16 v[80:95], a[188:191], v[184:187], v[80:95]\n"
    "ds_read_b64_tr_b16 a[204:205], v8 offset:49152\n"
    "ds_read_b64_tr_b16 a[206:207], v8 offset:49408\n"
    "s_add_u32 s82, s82, 0x1000\n"
    "buffer_load_dwordx4 v[224:227], v2, s[28:31], s82 offen sc1\n"
    "v_add_f32 v115, v115, v123\n"
    "s_waitcnt vmcnt(9) lgkmcnt(6)\n"
    "v_mfma_f32_32x32x16_bf16 v[64:79], a[192:195], v[188:191], v[64:79]\n"
    "ds_read_b64_tr_b16 a[176:177], v8 offset:17408\n"
    "ds_read_b64_tr_b16 a[178:179], v8 offset:17664\n"
    "v_add_f32 v112, v112, v124\n"
    "v_add_f32 v113, v113, v125\n"
    "v_add_f32 v114, v114, v126\n"
    "s_waitcnt lgkmcnt(6)\n"
    "v_mfma_f32_32x32x16_bf16 v[80:95], a[196:199], v[188:191], v[80:95]\n"
    "ds_read_b64_tr_b16 a[180:181], v8 offset:50176\n"
    "ds_read_b64_tr_b16 a[182:183], v8 offset:50432\n"
    "buffer_load_dwordx4 v[228:231], v2, s[28:31], s82 offen offset:1024 sc1\n"
    "v_add_f32 v115, v115, v127\n"
    "s_waitcnt vmcnt(9) lgkmcnt(6)\n"
    "v_mfma_f32_32x32x16_bf16 v[64:79], a[200:203], v[192:195], v[64:79]\n"
    "ds_read_b64_tr_b16 a[184:185], v8 offset:18432\n"
    "ds_read_b64_tr_b16 a[186:187], v8 offset:18688\n"
    "v_permlane16_swap_b32 v112, v114\n"
    "v_permlane16_swap_b32 v113, v115\n"
    "v_add_f32 v112, v112, v114\n"
    "s_waitcnt lgkmcnt(6)\n"
    "v_mfma_f32_32x32x16_bf16 v[80:95], a[204:207], v[192:195], v[80:95]\n"
    "ds_read_b64_tr_b16 a[188:189], v8 offset:51200\n"
    "ds_read_b64_tr_b16 a[190:191], v8 offset:51456\n"
    "buffer_load_dwordx4 v[232:235], v2, s[28:31], s82 offen offset:2048 sc1\n"
    "v_add_f32 v113, v113, v115\n"
    "s_waitcnt vmcnt(9) lgkmcnt(6)\n"
    "v_mfma_f32_32x32x16_bf16 v[64:79], a[176:179], v[196:199], v[64:79]\n"
    "ds_read_b64_tr_b16 a[192:193], v8 offset:19456\n"
    "ds_read_b64_tr_b16 a[194:195], v8 offset:19712\n"
    "v_mov_b32 v114, v112\n"
    "v_mov_b32 v115, v113\n"
    "s_nop 0\n"
    "v_permlane32_swap_b32 v112, v114\n"
    "s_waitcnt lgkmcnt(6)\n"
    "v_mfma_f32_32x32x16_bf16 v[80:95], a[180:183], v[196:199], v[80:95]\n"
    "ds_read_b64_tr_b16 a[196:197], v8 offset:52224\n"
    "ds_read_b64_tr_b16 a[198:199], v8 offset:52480\n"
    "buffer_load_dwordx4 v[236:239], v2, s[28:31], s82 offen offset:3072 sc1\n"
    "v_permlane32_swap_b32 v113, v115\n"
    "s_waitcnt vmcnt(9) lgkmcnt(6)\n"
    "v_mfma_f32_32x32x16_bf16 v[64:79], a[184:187], v[200:203], v[64:79]\n"
    "ds_read_b64_tr_b16 a[200:201], v8 offset:20480\n"
    "ds_read_b64_tr_b16 a[202:203], v8 offset:20736\n"
    "v_add_f32 v112, v112, v114\n"
    "v_add_f32 v113, v113, v115\n"
    "v_mul_f32 v112, 0xbb000000, v112\n"
    "s_waitcnt lgkmcnt(6)\n"
    "v_mfma_f32_32x32x16_bf16 v[80:95], a[188:191], v[200:203], v[80:95]\n"
    "ds_read_b64_tr_b16 a[204:205], v8 offset:53248\n"
    "ds_read_b64_tr_b16 a[206:207], v8 offset:53504\n"
    "s_add_u32 s82, s82, 0x1000\n"
    "buffer_load_dwordx4 v[240:243], v2, s[28:31], s82 offen sc1\n"
    "v_mul_f32 v114, v112, v112\n"
    "s_waitcnt vmcnt(9) lgkmcnt(6)\n"
    "v_mfma_f32_32x32x16_bf16 v[64:79], a[192:195], v[204:207], v[64:79]\n"
    "ds_read_b64_tr_b16 a[176:177], v8 offset:21504\n"
    "ds_read_b64_tr_b16 a[178:179], v8 offset:21760\n"
    "v_mul_f32 v113, 0x3b000000, v113\n"
    "v_sub_f32 v113, v113, v114\n"
    "v_max_f32 v113, 0, v113\n"
    "s_waitcnt lgkmcnt(6)\n"
    "v_mfma_f32_32x32x16_bf16 v[80:95], a[196:199], v[204:207], v[80:95]\n"
    "ds_read_b64_tr_b16 a[180:181], v8 offset:54272\n"
    "ds_read_b64_tr_b16 a[182:183], v8 offset:54528\n"
    "buffer_load_dwordx4 v[244:247], v2, s[28:31], s82 offen offset:1024 sc1\n"
    "v_add_f32 v113, 0x3727c5ac, v113\n"
    "s_waitcnt vmcnt(9) lgkmcnt(6)\n"
    "v_mfma_f32_32x32x16_bf16 v[64:79], a[200:203], v[208:211], v[64:79]\n"
    "ds_read_b64_tr_b16 a[184:185], v8 offset:22528\n"
    "ds_read_b64_tr_b16 a[186:187], v8 offset:22784\n"
    "v_rsq_f32 v116, v113\n"
    "v_cvt_pk_bf16_f32 v60, v112, v112\n"
    "v_mul_f32 v113, v113, v116\n"
    "s_waitcnt lgkmcnt(6)\n"
    "v_mfma_f32_32x32x16_bf16 v[80:95], a[204:207], v[208:211], v[80:95]\n"
    "ds_read_b64_tr_b16 a[188:189], v8 offset:55296\n"
    "ds_read_b64_tr_b16 a[190:191], v8 offset:55552\n"
    "buffer_load_dwordx4 v[248:251], v2, s[28:31], s82 offen offset:2048 sc1\n"
    "v_lshlrev_b32 v117, 16, v60\n"
    "s_waitcnt vmcnt(9) lgkmcnt(6)\n"
    "v_mfma_f32_32x32x16_bf16 v[64:79], a[176:179], v[212:215], v[64:79]\n"
    "ds_read_b64_tr_b16 a[192:193], v8 offset:23552\n"
    "ds_read_b64_tr_b16 a[194:195], v8 offset:23808\n"
    "v_sub_f32 v117, v112, v117\n"
    "v_cvt_pk_bf16_f32 v120, v113, v113\n"
    "v_lshlrev_b32 v118, 16, v120\n"
    "s_waitcnt lgkmcnt(6)\n"
    "v_mfma_f32_32x32x16_bf16 v[80:95], a[180:183], v[212:215], v[80:95]\n"
    "ds_read_b64_tr_b16 a[196:197], v8 offset:56320\n"
    "ds_read_b64_tr_b16 a[198:199], v8 offset:56576\n"
    "buffer_load_dwordx4 v[252:255], v2, s[28:31], s82 offen offset:3072 sc1\n"
    "v_sub_f32 v119, v113, v118\n"
    "s_waitcnt vmcnt(9) lgkmcnt(6)\n"
    "v_mfma_f32_32x32x16_bf16 v[64:79], a[184:187], v[216:219], v[64:79]\n"
    "ds_read_b64_tr_b16 a[200:201], v8 offset:24576\n"
    "ds_read_b64_tr_b16 a[202:203], v8 offset:24832\n"
    "v_cvt_pk_bf16_f32 v61, v117, v118\n"
    "v_cvt_pk_bf16_f32 v62, v118, v119\n"
    "v_mov_b32 v63, 0\n"
    "s_waitcnt lgkmcnt(6)\n"
    "v_mfma_f32_32x32x16_bf16 v[80:95], a[188:191], v[216:219], v[80:95]\n"
    "ds_read_b64_tr_b16 a[204:205], v8 offset:57344\n"
    "ds_read_b64_tr_b16 a[206:207], v8 offset:57600\n"
    "s_xor_b32 s76, s76, 0x1000\n"
    "buffer_load_dwordx4 a[112:115], v2, s[28:31], s76 offen sc1\n"
    "ds_write_b32 v28, v116\n"
    "s_waitcnt vmcnt(9) lgkmcnt(7)\n"
    "v_mfma_f32_32x32x16_bf16 v[64:79], a[192:195], v[220:223], v[64:79]\n"
    "ds_read_b64_tr_b16 a[176:177], v8 offset:25600\n"
    "ds_read_b64_tr_b16 a[178:179], v8 offset:25856\n"
    "s_waitcnt lgkmcnt(7)\n"
    "v_mfma_f32_32x32x16_bf16 v[80:95], a[196:199], v[220:223], v[80:95]\n"
    "ds_read_b64_tr_b16 a[180:181], v8 offset:58368\n"
    "ds_read_b64_tr_b16 a[182:183], v8 offset:58624\n"
    "buffer_load_dwordx4 a[116:119], v2, s[28:31], s76 offen offset:1024 sc1\n"
    "s_waitcnt vmcnt(9) lgkmcnt(7)\n"
    "v_mfma_f32_32x32x16_bf16 v[64:79], a[200:203], v[224:227], v[64:79]\n"
    "ds_read_b64_tr_b16 a[184:185], v8 offset:26624\n"
    "ds_read_b64_tr_b16 a[186:187], v8 offset:26880\n"
    "s_waitcnt lgkmcnt(7)\n"
    "v_mfma_f32_32x32x16_bf16 v[80:95], a[204:207], v[224:227], v[80:95]\n"
    "ds_read_b64_tr_b16 a[188:189], v8 offset:59392\n"
    "ds_read_b64_tr_b16 a[190:191], v8 offset:59648\n"
    "buffer_load_dwordx4 a[120:123], v2, s[28:31], s76 offen offset:2048 sc1\n"
    "s_waitcnt vmcnt(9) lgkmcnt(6)\n"
    "v_mfma_f32_32x32x16_bf16 v[64:79], a[176:179], v[228:231], v[64:79]\n"
    "ds_read_b64_tr_b16 a[192:193], v8 offset:27648\n"
    "ds_read_b64_tr_b16 a[194:195], v8 offset:27904\n"
    "s_waitcnt lgkmcnt(6)\n"
    "v_mfma_f32_32x32x16_bf16 v[80:95], a[180:183], v[228:231], v[80:95]\n"
    "ds_read_b64_tr_b16 a[196:197], v8 offset:60416\n"
    "ds_read_b64_tr_b16 a[198:199], v8 offset:60672\n"
    "buffer_load_dwordx4 a[124:127], v2, s[28:31], s76 offen offset:3072 sc1\n"
    "s_waitcnt vmcnt(9) lgkmcnt(6)\n"
    "v_mfma_f32_32x32x16_bf16 v[64:79], a[184:187], v[232:235], v[64:79]\n"
    "ds_read_b64_tr_b16 a[200:201], v8 offset:28672\n"
    "ds_read_b64_tr_b16 a[202:203], v8 offset:28928\n"
    "s_waitcnt lgkmcnt(6)\n"
    "v_mfma_f32_32x32x16_bf16 v[80:95], a[188:191], v[232:235], v[80:95]\n"
    "ds_read_b64_tr_b16 a[204:205], v8 offset:61440\n"
    "ds_read_b64_tr_b16 a[206:207], v8 offset:61696\n"
    "s_waitcnt vmcnt(8) lgkmcnt(6)\n"
    "v_mfma_f32_32x32x16_bf16 v[64:79], a[192:195], v[236:239], v[64:79]\n"
    "ds_read_b64_tr_b16 a[176:177], v8 offset:29696\n"
    "ds_read_b64_tr_b16 a[178:179], v8 offset:29952\n"
    "s_waitcnt lgkmcnt(6)\n"
    "v_mfma_f32_32x32x16_bf16 v[80:95], a[196:199], v[236:239], v[80:95]\n"
    "ds_read_b64_tr_b16 a[180:181], v8 offset:62464\n"
    "ds_read_b64_tr_b16 a[182:183], v8 offset:62720\n"
    "s_waitcnt vmcnt(7) lgkmcnt(6)\n"
    "v_mfma_f32_32x32x16_bf16 v[64:79], a[200:203], v[240:243], v[64:79]\n"
    "ds_read_b64_tr_b16 a[184:185], v8 offset:30720\n"
    "ds_read_b64_tr_b16 a[186:187], v8 offset:30976\n"
    "s_waitcnt lgkmcnt(6)\n"
    "v_mfma_f32_32x32x16_bf16 v[80:95], a[204:207], v[240:243], v[80:95]\n"
    "ds_read_b64_tr_b16 a[188:189], v8 offset:63488\n"
    "ds_read_b64_tr_b16 a[190:191], v8 offset:63744\n"
    "s_waitcnt vmcnt(6) lgkmcnt(6)\n"
    "v_mfma_f32_32x32x16_bf16 v[64:79], a[176:179], v[244:247], v[64:79]\n"
    "ds_read_b64_tr_b16 a[192:193], v8 offset:31744\n"
    "ds_read_b64_tr_b16 a[194:195], v8 offset:32000\n"
    "s_waitcnt lgkmcnt(6)\n"
    "v_mfma_f32_32x32x16_bf16 v[80:95], a[180:183], v[244:247], v[80:95]\n"
    "ds_read_b64_tr_b16 a[196:197], v8 offset:64512\n"
    "ds_read_b64_tr_b16 a[198:199], v8 offset:64768\n"
    "v_mfma_f32_32x32x16_bf16 v[64:79], v[48:51], v[60:63], v[64:79]\n"
    "v_mfma_f32_32x32x16_bf16 v[80:95], v[52:55], v[60:63], v[80:95]\n"
    "v_mfma_f32_32x32x16_bf16 v[96:111], v[56:59], v[60:63], v[96:111]\n"
    "s_waitcnt vmcnt(5) lgkmcnt(6)\n"
    "v_mfma_f32_32x32x16_bf16 v[64:79], a[184:187], v[248:251], v[64:79]\n"
    "ds_read_b64_tr_b16 a[200:201], v9 offset:6144\n"
    "ds_read_b64_tr_b16 a[202:203], v9 offset:6400\n"
    "s_waitcnt lgkmcnt(6)\n"
    "v_mfma_f32_32x32x16_bf16 v[80:95], a[188:191], v[248:251], v[80:95]\n"
    "ds_read_b64_tr_b16 a[204:205], v9 offset:7168\n"
    "ds_read_b64_tr_b16 a[206:207], v9 offset:7424\n"
    "s_waitcnt vmcnt(4) lgkmcnt(6)\n"
    "v_mfma_f32_32x32x16_bf16 v[64:79], a[192:195], v[252:255], v[64:79]\n"
    "ds_read_b64_tr_b16 a[176:177], v9 offset:8192\n"
    "ds_read_b64_tr_b16 a[178:179], v9 offset:8448\n"
    "s_waitcnt lgkmcnt(6)\n"
    "v_mfma_f32_32x32x16_bf16 v[80:95], a[196:199], v[252:255], v[80:95]\n"
    "ds_read_b64_tr_b16 a[180:181], v9 offset:9216\n"
    "ds_read_b64_tr_b16 a[182:183], v9 offset:9472\n"
    "s_waitcnt lgkmcnt(6)\n"
    "v_mfma_f32_32x32x16_bf16 v[96:111], a[200:203], v[152:155], v[96:111]\n"
    "ds_read_b64_tr_b16 a[184:185], v9 offset:10240\n"
    "ds_read_b64_tr_b16 a[186:187], v9 offset:10496\n"
    "s_waitcnt lgkmcnt(6)\n"
    "v_mfma_f32_32x32x16_bf16 v[96:111], a[204:207], v[156:159], v[96:111]\n"
    "ds_read_b64_tr_b16 a[188:189], v9 offset:11264\n"
    "ds_read_b64_tr_b16 a[190:191], v9 offset:11520\n"
    "v_cvt_pk_bf16_f32 v64, v64, v65\n"
    "v_cvt_pk_bf16_f32 v65, v66, v67\n"
    "v_cvt_pk_bf16_f32 v66, v68, v69\n"
    "v_cvt_pk_bf16_f32 v67, v70, v71\n"
    "s_waitcnt lgkmcnt(6)\n"
    "v_mfma_f32_32x32x16_bf16 v[96:111], a[176:179], v[160:163], v[96:111]\n"
    "ds_read_b64_tr_b16 a[192:193], v9 offset:12288\n"
    "ds_read_b64_tr_b16 a[194:195], v9 offset:12544\n"
    "v_pk_max_i16 v64, v64, 0\n"
    "v_pk_max_i16 v65, v65, 0\n"
    "v_pk_max_i16 v66, v66, 0\n"
    "v_pk_max_i16 v67, v67, 0\n"
    "s_waitcnt lgkmcnt(6)\n"
    "v_mfma_f32_32x32x16_bf16 v[96:111], a[180:183], v[164:167], v[96:111]\n"
    "ds_read_b64_tr_b16 a[196:197], v9 offset:13312\n"
    "ds_read_b64_tr_b16 a[198:199], v9 offset:13568\n"
    "buffer_store_dwordx4 v[64:67], v2, s[28:31], s84 offen sc1\n"
    "v_cvt_pk_bf16_f32 v72, v72, v73\n"
    "v_cvt_pk_bf16_f32 v73, v74, v75\n"
    "v_cvt_pk_bf16_f32 v74, v76, v77\n"
    "s_waitcnt lgkmcnt(6)\n"
    "v_mfma_f32_32x32x16_bf16 v[96:111], a[184:187], v[168:171], v[96:111]\n"
    "ds_read_b64_tr_b16 a[200:201], v9 offset:14336\n"
    "ds_read_b64_tr_b16 a[202:203], v9 offset:14592\n"
    "v_cvt_pk_bf16_f32 v75, v78, v79\n"
    "v_pk_max_i16 v72, v72, 0\n"
    "v_pk_max_i16 v73, v73, 0\n"
    "v_pk_max_i16 v74, v74, 0\n"
    "s_waitcnt lgkmcnt(6)\n"
    "v_mfma_f32_32x32x16_bf16 v[96:111], a[188:191], v[172:175], v[96:111]\n"
    "ds_read_b64_tr_b16 a[204:205], v9 offset:15360\n"
    "ds_read_b64_tr_b16 a[206:207], v9 offset:15616\n"
    "v_pk_max_i16 v75, v75, 0\n"
    "buffer_store_dwordx4 v[72:75], v2, s[28:31], s84 offen offset:1024 sc1\n"
    "v_cvt_pk_bf16_f32 v80, v80, v81\n"
    "v_cvt_pk_bf16_f32 v81, v82, v83\n"
    "s_waitcnt lgkmcnt(6)\n"
    "v_mfma_f32_32x32x16_bf16 v[96:111], a[192:195], v[176:179], v[96:111]\n"
    "ds_read_b64_tr_b16 a[176:177], v9 offset:16384\n"
    "ds_read_b64_tr_b16 a[178:179], v9 offset:16640\n"
    "v_cvt_pk_bf16_f32 v82, v84, v85\n"
    "v_cvt_pk_bf16_f32 v83, v86, v87\n"
    "v_pk_max_i16 v80, v80, 0\n"
    "v_pk_max_i16 v81, v81, 0\n"
    "s_waitcnt lgkmcnt(6)\n"
    "v_mfma_f32_32x32x16_bf16 v[96:111], a[196:199], v[180:183], v[96:111]\n"
    "ds_read_b64_tr_b16 a[180:181], v9 offset:17408\n"
    "ds_read_b64_tr_b16 a[182:183], v9 offset:17664\n"
    "v_pk_max_i16 v82, v82, 0\n"
    "v_pk_max_i16 v83, v83, 0\n"
    "buffer_store_dwordx4 v[80:83], v2, s[28:31], s84 offen offset:2048 sc1\n"
    "v_cvt_pk_bf16_f32 v88, v88, v89\n"
    "s_waitcnt lgkmcnt(6)\n"
    "v_mfma_f32_32x32x16_bf16 v[96:111], a[200:203], v[184:187], v[96:111]\n"
    "ds_read_b64_tr_b16 a[184:185], v9 offset:18432\n"
    "ds_read_b64_tr_b16 a[186:187], v9 offset:18688\n"
    "v_cvt_pk_bf16_f32 v89, v90, v91\n"
    "v_cvt_pk_bf16_f32 v90, v92, v93\n"
    "v_cvt_pk_bf16_f32 v91, v94, v95\n"
    "v_pk_max_i16 v88, v88, 0\n"
    "s_waitcnt lgkmcnt(6)\n"
    "v_mfma_f32_32x32x16_bf16 v[96:111], a[204:207], v[188:191], v[96:111]\n"
    "ds_read_b64_tr_b16 a[188:189], v9 offset:19456\n"
    "ds_read_b64_tr_b16 a[190:191], v9 offset:19712\n"
    "v_pk_max_i16 v89, v89, 0\n"
    "v_pk_max_i16 v90, v90, 0\n"
    "v_pk_max_i16 v91, v91, 0\n"
    "buffer_store_dwordx4 v[88:91], v2, s[28:31], s84 offen offset:3072 sc1\n"
    "s_waitcnt lgkmcnt(6)\n"
    "v_mfma_f32_32x32x16_bf16 v[96:111], a[176:179], v[192:195], v[96:111]\n"
    "ds_read_b64_tr_b16 a[192:193], v9 offset:20480\n"
    "ds_read_b64_tr_b16 a[194:195], v9 offset:20736\n"
    "s_waitcnt lgkmcnt(6)\n"
    "v_mfma_f32_32x32x16_bf16 v[96:111], a[180:183], v[196:199], v[96:111]\n"
    "ds_read_b64_tr_b16 a[196:197], v9 offset:21504\n"
    "ds_read_b64_tr_b16 a[198:199], v9 offset:21760\n"
    "buffer_load_dwordx4 a[128:131], v2, s[28:31], s77 offen sc1\n"
    "s_waitcnt lgkmcnt(6)\n"
    "v_mfma_f32_32x32x16_bf16 v[96:111], a[184:187], v[200:203], v[96:111]\n"
    "ds_read_b64_tr_b16 a[200:201], v9 offset:22528\n"
    "ds_read_b64_tr_b16 a[202:203], v9 offset:22784\n"
    "s_waitcnt lgkmcnt(6)\n"
    "v_mfma_f32_32x32x16_bf16 v[96:111], a[188:191], v[204:207], v[96:111]\n"
    "ds_read_b64_tr_b16 a[204:205], v9 offset:23552\n"
    "ds_read_b64_tr_b16 a[206:207], v9 offset:23808\n"
    "buffer_load_dwordx4 a[132:135], v2, s[28:31], s77 offen offset:1024 sc1\n"
    "s_waitcnt lgkmcnt(6)\n"
    "v_mfma_f32_32x32x16_bf16 v[96:111], a[192:195], v[208:211], v[96:111]\n"
    "ds_read_b64_tr_b16 a[176:177], v9 offset:24576\n"
    "ds_read_b64_tr_b16 a[178:179], v9 offset:24832\n"
    "s_waitcnt lgkmcnt(6)\n"
    "v_mfma_f32_32x32x16_bf16 v[96:111], a[196:199], v[212:215], v[96:111]\n"
    "ds_read_b64_tr_b16 a[180:181], v9 offset:25600\n"
    "ds_read_b64_tr_b16 a[182:183], v9 offset:25856\n"
    "buffer_load_dwordx4 a[136:139], v2, s[28:31], s77 offen offset:2048 sc1\n"
    "s_waitcnt lgkmcnt(6)\n"
    "v_mfma_f32_32x32x16_bf16 v[96:111], a[200:203], v[216:219], v[96:111]\n"
    "ds_read_b64_tr_b16 a[184:185], v9 offset:26624\n"
    "ds_read_b64_tr_b16 a[186:187], v9 offset:26880\n"
    "s_waitcnt lgkmcnt(6)\n"
    "v_mfma_f32_32x32x16_bf16 v[96:111], a[204:207], v[220:223], v[96:111]\n"
    "ds_read_b64_tr_b16 a[188:189], v9 offset:27648\n"
    "ds_read_b64_tr_b16 a[190:191], v9 offset:27904\n"
    "buffer_load_dwordx4 a[140:143], v2, s[28:31], s77 offen offset:3072 sc1\n"
    "s_waitcnt lgkmcnt(6)\n"
    "v_mfma_f32_32x32x16_bf16 v[96:111], a[176:179], v[224:227], v[96:111]\n"
    "ds_read_b64_tr_b16 a[192:193], v9 offset:28672\n"
    "ds_read_b64_tr_b16 a[194:195], v9 offset:28928\n"
    "s_waitcnt lgkmcnt(6)\n"
    "v_mfma_f32_32x32x16_bf16 v[96:111], a[180:183], v[228:231], v[96:111]\n"
    "ds_read_b64_tr_b16 a[196:197], v9 offset:29696\n"
    "ds_read_b64_tr_b16 a[198:199], v9 offset:29952\n"
    "s_add_u32 s77, s77, 0x1000\n"
    "buffer_load_dwordx4 a[144:147], v2, s[28:31], s77 offen sc1\n"
    "s_waitcnt lgkmcnt(6)\n"
    "v_mfma_f32_32x32x16_bf16 v[96:111], a[184:187], v[232:235], v[96:111]\n"
    "ds_read_b64_tr_b16 a[200:201], v9 offset:30720\n"
    "ds_read_b64_tr_b16 a[202:203], v9 offset:30976\n"
    "s_waitcnt lgkmcnt(6)\n"
    "v_mfma_f32_32x32x16_bf16 v[96:111], a[188:191], v[236:239], v[96:111]\n"
    "ds_read_b64_tr_b16 a[204:205], v9 offset:31744\n"
    "ds_read_b64_tr_b16 a[206:207], v9 offset:32000\n"
    "buffer_load_dwordx4 a[148:151], v2, s[28:31], s77 offen offset:1024 sc1\n"
    "s_waitcnt lgkmcnt(6)\n"
    "v_mfma_f32_32x32x16_bf16 v[96:111], a[192:195], v[240:243], v[96:111]\n"
    "s_waitcnt lgkmcnt(4)\n"
    "v_mfma_f32_32x32x16_bf16 v[96:111], a[196:199], v[244:247], v[96:111]\n"
    "buffer_load_dwordx4 a[152:155], v2, s[28:31], s77 offen offset:2048 sc1\n"
    "s_waitcnt lgkmcnt(2)\n"
    "v_mfma_f32_32x32x16_bf16 v[96:111], a[200:203], v[248:251], v[96:111]\n"
    "s_waitcnt lgkmcnt(0)\n"
    "v_mfma_f32_32x32x16_bf16 v[96:111], a[204:207], v[252:255], v[96:111]\n"
    "buffer_load_dwordx4 a[156:159], v2, s[28:31], s77 offen offset:3072 sc1\n"
    "ds_read_b32 v112, v24\n"
    "s_nop 9\n"
    "v_cvt_pk_bf16_f32 v96, v96, v97\n"
    "v_cvt_pk_bf16_f32 v97, v98, v99\n"
    "v_cvt_pk_bf16_f32 v98, v100, v101\n"
    "v_cvt_pk_bf16_f32 v99, v102, v103\n"
    "v_pk_max_i16 v96, v96, 0\n"
    "v_pk_max_i16 v97, v97, 0\n"
    "v_pk_max_i16 v98, v98, 0\n"
    "v_pk_max_i16 v99, v99, 0\n"
    "s_waitcnt lgkmcnt(0)\n"
    "s_waitcnt vmcnt(8)\n"
    "s_barrier\n"
    "s_cmp_lg_u32 s15, 2\n"
    "s_cbranch_scc1 .Ls15\n"
    "s_mov_b64 exec, 0xffff\n"
    "buffer_store_dwordx2 v[6:7], v19, s[28:31], s62 offen sc1\n"
    "s_mov_b64 exec, -1\n"
    ".Ls15:\n"
    "s_add_u32 s62, s62, 0x20000\n"
    "buffer_store_dwordx4 v[96:99], v20, s[28:31], s85 offen sc1\n"
    "v_cvt_pk_bf16_f32 v104, v104, v105\n"
    "v_cvt_pk_bf16_f32 v105, v106, v107\n"
    "v_cvt_pk_bf16_f32 v106, v108, v109\n"
    "v_cvt_pk_bf16_f32 v107, v110, v111\n"
    "v_pk_max_i16 v104, v104, 0\n"
    "v_pk_max_i16 v105, v105, 0\n"
    "v_pk_max_i16 v106, v106, 0\n"
    "v_pk_max_i16 v107, v107, 0\n"
    "buffer_store_dwordx4 v[104:107], v20, s[28:31], s85 offen offset:1024 sc1\n"
    "s_add_u32 s77, s77, 0x1000\n"
    "buffer_load_dwordx4 a[160:163], v2, s[28:31], s77 offen sc1\n"
    "buffer_load_dwordx4 a[164:167], v2, s[28:31], s77 offen offset:1024 sc1\n"
    "buffer_load_dwordx4 a[168:171], v2, s[28:31], s77 offen offset:2048 sc1\n"
    "buffer_load_dwordx4 a[172:175], v2, s[28:31], s77 offen offset:3072 sc1\n"
    "ds_read_b128 v[116:119], v29\n"
    "ds_read_b128 v[120:123], v29 offset:32\n"
    "ds_read_b32 v32, v22\n"
    "ds_read_b32 v33, v22 offset:144\n"
    "ds_read_b32 v34, v22 offset:288\n"
    "ds_read_b32 v35, v22 offset:432\n"
    "ds_read_b32 v36, v22 offset:4608\n"
    "ds_read_b32 v37, v22 offset:4752\n"
    "ds_read_b32 v38, v22 offset:4896\n"
    "ds_read_b32 v39, v22 offset:5040\n"
    "ds_read_b32 v40, v22 offset:1152\n"
    "ds_read_b32 v41, v22 offset:1296\n"
    "ds_read_b32 v42, v22 offset:1440\n"
    "ds_read_b32 v43, v22 offset:1584\n"
    "ds_read_b32 v44, v22 offset:5760\n"
    "ds_read_b32 v45, v22 offset:5904\n"
    "ds_read_b32 v46, v22 offset:6048\n"
    "ds_read_b32 v47, v22 offset:6192\n"
    "v_lshlrev_b32 v114, 16, v112\n"
    "v_and_b32 v115, s75, v112\n"
    "s_waitcnt lgkmcnt(15)\n"
    "v_pk_mul_f32 v[48:49], v[116:117], v[114:115] op_sel_hi:[1,0]\n"
    "v_pk_mul_f32 v[50:51], v[118:119], v[114:115] op_sel_hi:[1,0]\n"
    "v_pk_mul_f32 v[52:53], v[116:117], v[114:115] op_sel:[0,1]\n"
    "v_pk_mul_f32 v[54:55], v[118:119], v[114:115] op_sel:[0,1]\n"
    "v_pk_mul_f32 v[56:57], v[120:121], v[114:115] op_sel_hi:[1,0]\n"
    "v_pk_mul_f32 v[58:59], v[122:123], v[114:115] op_sel_hi:[1,0]\n"
    "v_pk_mul_f32 v[60:61], v[120:121], v[114:115] op_sel:[0,1]\n"
    "v_pk_mul_f32 v[62:63], v[122:123], v[114:115] op_sel:[0,1]\n"
    "s_waitcnt lgkmcnt(0)\n"
    "s_sleep 3\n"
    "s_cmp_lg_u32 s15, 0\n"
    "s_cbranch_scc1 .Lr16\n"
    "v_not_b32 v31, v7\n"
    "s_mov_b64 exec, 0xffff\n"
    "buffer_load_dwordx2 v[30:31], v3, s[28:31], s64 offen sc1\n"
    "s_mov_b64 exec, -1\n"
    ".Lr16:\n"
    "s_waitcnt vmcnt(4)\n"
    "s_barrier\n"
    "s_cmp_lg_u32 s15, 1\n"
    "s_cbranch_scc1 .Ls17\n"
    "s_mov_b64 exec, 0xffff\n"
    "buffer_store_dwordx2 v[6:7], v19, s[28:31], s62 offen sc1\n"
    "s_mov_b64 exec, -1\n"
    ".Ls17:\n"
    "s_cmp_lg_u32 s15, 0\n"
    "s_cbranch_scc1 .Lr18\n"
    "s_mov_b64 exec, 0xffff\n"
    "s_waitcnt vmcnt(0)\n"
    "v_cmp_ne_u64 vcc, s[12:13], v[30:31]\n"
    "s_cbranch_vccz .Lq18\n"
    ".Lp18:\n"
    "buffer_load_dwordx2 v[30:31], v3, s[28:31], s64 offen sc1\n"
    "s_sub_i32 s14, s14, 1\n"
    "s_cmp_lt_i32 s14, 0\n"
    "s_waitcnt vmcnt(0)\n"
    "s_cbranch_scc1 .Lq18\n"
    "v_cmp_ne_u64 vcc, s[12:13], v[30:31]\n"
    "s_cbranch_vccnz .Lp18\n"
    ".Lq18:\n"
    "s_mov_b64 exec, -1\n"
    ".Lr18:\n"
    "s_barrier\n"
    "s_add_u32 s64, s64, 0x20000\n"
    "buffer_load_dwordx4 v[128:131], v12, s[28:31], s16 offen sc1\n"
    "s_nop 10\n"
    "buffer_load_dwordx4 v[132:135], v12, s[28:31], s16 offen offset:1024 sc1\n"
    "s_nop 10\n"
    "buffer_load_dwordx4 v[136:139], v12, s[28:31], s16 offen offset:2048 sc1\n"
    "s_nop 10\n"
    "buffer_load_dwordx4 v[140:143], v12, s[28:31], s16 offen offset:3072 sc1\n"
    "s_nop 10\n"
    "s_add_u32 s4, s16, 0x1000\n"
    "buffer_load_dwordx4 v[144:147], v12, s[28:31], s4 offen sc1\n"
    "s_nop 9\n"
    "buffer_load_dwordx4 v[148:151], v12, s[28:31], s4 offen offset:1024 sc1\n"
    "s_nop 10\n"
    "buffer_load_dwordx4 v[152:155], v12, s[28:31], s4 offen offset:2048 sc1\n"
    "s_nop 10\n"
    "buffer_load_dwordx4 v[156:159], v12, s[28:31], s4 offen offset:3072 sc1\n"
    "s_nop 10\n"
    "s_add_u32 s4, s16, 0x2000\n"
    "buffer_load_dwordx4 v[160:163], v12, s[28:31], s4 offen sc1\n"
    "s_nop 9\n"
    "buffer_load_dwordx4 v[164:167], v12, s[28:31], s4 offen offset:1024 sc1\n"
    "s_waitcnt vmcnt(9)\n"
    "v_mfma_f32_32x32x16_bf16 v[64:79], v[128:131], a[0:3], 0\n"
    "v_mfma_f32_32x32x16_bf16 v[80:95], v[128:131], a[4:7], 0\n"
    "buffer_load_dwordx4 v[168:171], v12, s[28:31], s4 offen offset:2048 sc1\n"
    "s_waitcnt vmcnt(9)\n"
    "v_mfma_f32_32x32x16_bf16 v[64:79], v[132:135], a[8:11], v[64:79]\n"
    "v_mfma_f32_32x32x16_bf16 v[80:95], v[132:135], a[12:15], v[80:95]\n"
    "buffer_load_dwordx4 v[172:175], v12, s[28:31], s4 offen offset:3072 sc1\n"
    "s_waitcnt vmcnt(9)\n"
    "v_mfma_f32_32x32x16_bf16 v[64:79], v[136:139], a[16:19], v[64:79]\n"
    "v_mfma_f32_32x32x16_bf16 v[80:95], v[136:139], a[20:23], v[80:95]\n"
    "s_add_u32 s4, s16, 0x3000\n"
    "buffer_load_dwordx4 v[176:179], v12, s[28:31], s4 offen sc1\n"
    "s_waitcnt vmcnt(9)\n"
    "v_mfma_f32_32x32x16_bf16 v[64:79], v[140:143], a[24:27], v[64:79]\n"
    "v_mfma_f32_32x32x16_bf16 v[80:95], v[140:143], a[28:31], v[80:95]\n"
    "buffer_load_dwordx4 v[180:183], v12, s[28:31], s4 offen offset:1024 sc1\n"
    "s_waitcnt vmcnt(9)\n"
    "v_mfma_f32_32x32x16_bf16 v[64:79], v[144:147], a[32:35], v[64:79]\n"
    "v_mfma_f32_32x32x16_bf16 v[80:95], v[144:147], a[36:39], v[80:95]\n"
    "buffer_load_dwordx4 v[184:187], v12, s[28:31], s4 offen offset:2048 sc1\n"
    "s_waitcnt vmcnt(9)\n"
    "v_mfma_f32_32x32x16_bf16 v[64:79], v[148:151], a[40:43], v[64:79]\n"
    "v_mfma_f32_32x32x16_bf16 v[80:95], v[148:151], a[44:47], v[80:95]\n"
    "buffer_load_dwordx4 v[188:191], v12, s[28:31], s4 offen offset:3072 sc1\n"
    "s_waitcnt vmcnt(9)\n"
    "v_mfma_f32_32x32x16_bf16 v[64:79], v[152:155], a[48:51], v[64:79]\n"
    "v_mfma_f32_32x32x16_bf16 v[80:95], v[152:155], a[52:55], v[80:95]\n"
    "s_mov_b32 exec_lo, s37\n"
    "s_mov_b32 exec_hi, 0\n"
    "buffer_load_dwordx2 v[30:31], v3, s[28:31], s64 offen sc1\n"
    "s_mov_b64 exec, -1\n"
    "buffer_load_dwordx4 v[192:195], v12, s[28:31], s17 offen sc1\n"
    "s_waitcnt vmcnt(10)\n"
    "v_mfma_f32_32x32x16_bf16 v[64:79], v[156:159], a[56:59], v[64:79]\n"
    "v_mfma_f32_32x32x16_bf16 v[80:95], v[156:159], a[60:63], v[80:95]\n"
    "buffer_load_dwordx4 v[196:199], v12, s[28:31], s17 offen offset:1024 sc1\n"
    "s_waitcnt vmcnt(10)\n"
    "v_mfma_f32_32x32x16_bf16 v[64:79], v[160:163], a[64:67], v[64:79]\n"
    "v_mfma_f32_32x32x16_bf16 v[80:95], v[160:163], a[68:71], v[80:95]\n"
    "buffer_load_dwordx4 v[200:203], v12, s[28:31], s17 offen offset:2048 sc1\n"
    "s_waitcnt vmcnt(10)\n"
    "v_mfma_f32_32x32x16_bf16 v[64:79], v[164:167], a[72:75], v[64:79]\n"
    "v_mfma_f32_32x32x16_bf16 v[80:95], v[164:167], a[76:79], v[80:95]\n"
    "buffer_load_dwordx4 v[204:207], v12, s[28:31], s17 offen offset:3072 sc1\n"
    "s_waitcnt vmcnt(10)\n"
    "v_mfma_f32_32x32x16_bf16 v[64:79], v[168:171], a[80:83], v[64:79]\n"
    "v_mfma_f32_32x32x16_bf16 v[80:95], v[168:171], a[84:87], v[80:95]\n"
    "s_add_u32 s4, s17, 0x1000\n"
    "buffer_load_dwordx4 v[208:211], v12, s[28:31], s4 offen sc1\n"
    "s_waitcnt vmcnt(10)\n"
    "v_mfma_f32_32x32x16_bf16 v[64:79], v[172:175], a[88:91], v[64:79]\n"
    "v_mfma_f32_32x32x16_bf16 v[80:95], v[172:175], a[92:95], v[80:95]\n"
    "buffer_load_dwordx4 v[212:215], v12, s[28:31], s4 offen offset:1024 sc1\n"
    "s_waitcnt vmcnt(10)\n"
    "v_mfma_f32_32x32x16_bf16 v[64:79], v[176:179], a[96:99], v[64:79]\n"
    "v_mfma_f32_32x32x16_bf16 v[80:95], v[176:179], a[100:103], v[80:95]\n"
    "buffer_load_dwordx4 v[216:219], v12, s[28:31], s4 offen offset:2048 sc1\n"
    "s_waitcnt vmcnt(10)\n"
    "v_mfma_f32_32x32x16_bf16 v[64:79], v[180:183], a[104:107], v[64:79]\n"
    "v_mfma_f32_32x32x16_bf16 v[80:95], v[180:183], a[108:111], v[80:95]\n"
    "buffer_load_dwordx4 v[220:223], v12, s[28:31], s4 offen offset:3072 sc1\n"
    "s_waitcnt vmcnt(10)\n"
    "v_mfma_f32_32x32x16_bf16 v[64:79], v[184:187], a[112:115], v[64:79]\n"
    "v_mfma_f32_32x32x16_bf16 v[80:95], v[184:187], a[116:119], v[80:95]\n"
    "s_add_u32 s4, s17, 0x2000\n"
    "buffer_load_dwordx4 v[128:131], v12, s[28:31], s4 offen sc1\n"
    "s_waitcnt vmcnt(10)\n"
    "v_mfma_f32_32x32x16_bf16 v[64:79], v[188:191], a[120:123], v[64:79]\n"
    "v_mfma_f32_32x32x16_bf16 v[80:95], v[188:191], a[124:127], v[80:95]\n"
    "buffer_load_dwordx4 v[132:135], v12, s[28:31], s4 offen offset:1024 sc1\n"
    "s_waitcnt vmcnt(9)\n"
    "v_mfma_f32_32x32x16_bf16 v[96:111], v[192:195], a[0:3], 0\n"
    "v_mfma_f32_32x32x16_bf16 v[112:127], v[192:195], a[4:7], 0\n"
    "s_mov_b32 exec_lo, s37\n"
    "s_mov_b32 exec_hi, 0\n"
    "v_cmp_ne_u64 vcc, s[12:13], v[30:31]\n"
    "s_cbranch_vccz .Lq19\n"
    ".Lp19:\n"
    "buffer_load_dwordx2 v[30:31], v3, s[28:31], s64 offen sc1\n"
    "s_sub_i32 s14, s14, 1\n"
    "s_cmp_lt_i32 s14, 0\n"
    "s_waitcnt vmcnt(0)\n"
    "s_cbranch_scc1 .Lq19\n"
    "v_cmp_ne_u64 vcc, s[12:13], v[30:31]\n"
    "s_cbranch_vccnz .Lp19\n"
    ".Lq19:\n"
    "s_mov_b64 exec, -1\n"
    "buffer_load_dwordx4 v[136:139], v12, s[28:31], s18 offen sc1\n"
    "s_waitcnt vmcnt(9)\n"
    "v_mfma_f32_32x32x16_bf16 v[96:111], v[196:199], a[8:11], v[96:111]\n"
    "v_mfma_f32_32x32x16_bf16 v[112:127], v[196:199], a[12:15], v[112:127]\n"
    "buffer_load_dwordx4 v[140:143], v12, s[28:31], s18 offen offset:1024 sc1\n"
    "s_waitcnt vmcnt(9)\n"
    "v_mfma_f32_32x32x16_bf16 v[96:111], v[200:203], a[16:19], v[96:111]\n"
    "v_mfma_f32_32x32x16_bf16 v[112:127], v[200:203], a[20:23], v[112:127]\n"
    "buffer_load_dwordx4 v[144:147], v12, s[28:31], s18 offen offset:2048 sc1\n"
    "s_waitcnt vmcnt(9)\n"
    "v_mfma_f32_32x32x16_bf16 v[96:111], v[204:207], a[24:27], v[96:111]\n"
    "v_mfma_f32_32x32x16_bf16 v[112:127], v[204:207], a[28:31], v[112:127]\n"
    "buffer_load_dwordx4 v[148:151], v12, s[28:31], s18 offen offset:3072 sc1\n"
    "s_waitcnt vmcnt(9)\n"
    "v_mfma_f32_32x32x16_bf16 v[96:111], v[208:211], a[32:35], v[96:111]\n"
    "v_mfma_f32_32x32x16_bf16 v[112:127], v[208:211], a[36:39], v[112:127]\n"
    "s_add_u32 s4, s18, 0x1000\n"
    "buffer_load_dwordx4 v[152:155], v21, s[28:31], s4 offen sc1\n"
    "s_waitcnt vmcnt(9)\n"
    "v_mfma_f32_32x32x16_bf16 v[96:111], v[212:215], a[40:43], v[96:111]\n"
    "v_mfma_f32_32x32x16_bf16 v[112:127], v[212:215], a[44:47], v[112:127]\n"
    "buffer_load_dwordx4 v[156:159], v21, s[28:31], s4 offen offset:1024 sc1\n"
    "s_waitcnt vmcnt(9)\n"
    "v_mfma_f32_32x32x16_bf16 v[96:111], v[216:219], a[48:51], v[96:111]\n"
    "v_mfma_f32_32x32x16_bf16 v[112:127], v[216:219], a[52:55], v[112:127]\n"
    "s_add_u32 s4, s17, 0x2000\n"
    "buffer_load_dwordx4 v[160:163], v12, s[28:31], s4 offen offset:2048 sc1\n"
    "s_waitcnt vmcnt(9)\n"
    "v_mfma_f32_32x32x16_bf16 v[96:111], v[220:223], a[56:59], v[96:111]\n"
    "v_mfma_f32_32x32x16_bf16 v[112:127], v[220:223], a[60:63], v[112:127]\n"
    "buffer_load_dwordx4 v[164:167], v12, s[28:31], s4 offen offset:3072 sc1\n"
    "s_waitcnt vmcnt(9)\n"
    "v_mfma_f32_32x32x16_bf16 v[96:111], v[128:131], a[64:67], v[96:111]\n"
    "v_mfma_f32_32x32x16_bf16 v[112:127], v[128:131], a[68:71], v[112:127]\n"
    "s_add_u32 s4, s17, 0x3000\n"
    "buffer_load_dwordx4 v[168:171], v12, s[28:31], s4 offen sc1\n"
    "s_waitcnt vmcnt(9)\n"
    "v_mfma_f32_32x32x16_bf16 v[96:111], v[132:135], a[72:75], v[96:111]\n"
    "v_mfma_f32_32x32x16_bf16 v[112:127], v[132:135], a[76:79], v[112:127]\n"
    "buffer_load_dwordx4 v[172:175], v12, s[28:31], s4 offen offset:1024 sc1\n"
    "s_waitcnt vmcnt(9)\n"
    "v_mfma_f32_32x32x16_bf16 v[64:79], v[136:139], a[128:131], v[64:79]\n"
    "v_mfma_f32_32x32x16_bf16 v[80:95], v[136:139], a[132:135], v[80:95]\n"
    "buffer_load_dwordx4 v[176:179], v12, s[28:31], s4 offen offset:2048 sc1\n"
    "s_waitcnt vmcnt(9)\n"
    "v_mfma_f32_32x32x16_bf16 v[64:79], v[140:143], a[136:139], v[64:79]\n"
    "v_mfma_f32_32x32x16_bf16 v[80:95], v[140:143], a[140:143], v[80:95]\n"
    "buffer_load_dwordx4 v[180:183], v12, s[28:31], s4 offen offset:3072 sc1\n"
    "s_waitcnt vmcnt(9)\n"
    "v_mfma_f32_32x32x16_bf16 v[64:79], v[144:147], a[144:147], v[64:79]\n"
    "v_mfma_f32_32x32x16_bf16 v[80:95], v[144:147], a[148:151], v[80:95]\n"
    "buffer_load_dwordx4 v[184:187], v12, s[28:31], s19 offen sc1\n"
    "s_waitcnt vmcnt(9)\n"
    "v_mfma_f32_32x32x16_bf16 v[64:79], v[148:151], a[152:155], v[64:79]\n"
    "v_mfma_f32_32x32x16_bf16 v[80:95], v[148:151], a[156:159], v[80:95]\n"
    "buffer_load_dwordx4 v[188:191], v12, s[28:31], s19 offen offset:1024 sc1\n"
    "s_waitcnt vmcnt(9)\n"
    "v_mfma_f32_32x32x16_bf16 v[64:79], v[152:155], a[160:163], v[64:79]\n"
    "v_mfma_f32_32x32x16_bf16 v[80:95], v[152:155], a[164:167], v[80:95]\n"
    "buffer_load_dwordx4 v[192:195], v12, s[28:31], s19 offen offset:2048 sc1\n"
    "s_waitcnt vmcnt(9)\n"
    "v_mfma_f32_32x32x16_bf16 v[64:79], v[156:159], a[168:171], v[64:79]\n"
    "v_mfma_f32_32x32x16_bf16 v[80:95], v[156:159], a[172:175], v[80:95]\n"
    "buffer_load_dwordx4 v[196:199], v12, s[28:31], s19 offen offset:3072 sc1\n"
    "s_waitcnt vmcnt(9)\n"
    "v_mfma_f32_32x32x16_bf16 v[96:111], v[160:163], a[80:83], v[96:111]\n"
    "v_mfma_f32_32x32x16_bf16 v[112:127], v[160:163], a[84:87], v[112:127]\n"
    "s_add_u32 s4, s19, 0x1000\n"
    "buffer_load_dwordx4 v[200:203], v21, s[28:31], s4 offen sc1\n"
    "s_waitcnt vmcnt(9)\n"
    "v_mfma_f32_32x32x16_bf16 v[96:111], v[164:167], a[88:91], v[96:111]\n"
    "s_nop 2\n"
    "ds_write_b128 v16, v[64:67]\n"
    "v_mfma_f32_32x32x16_bf16 v[112:127], v[164:167], a[92:95], v[112:127]\n"
    "ds_write_b128 v16, v[80:83] offset:1024\n"
    "buffer_load_dwordx4 v[204:207], v21, s[28:31], s4 offen offset:1024 sc1\n"
    "s_waitcnt vmcnt(9)\n"
    "v_mfma_f32_32x32x16_bf16 v[96:111], v[168:171], a[96:99], v[96:111]\n"
    "ds_write_b128 v16, v[68:71] offset:2048\n"
    "v_mfma_f32_32x32x16_bf16 v[112:127], v[168:171], a[100:103], v[112:127]\n"
    "ds_write_b128 v16, v[84:87] offset:3072\n"
    "s_waitcnt vmcnt(8)\n"
    "v_mfma_f32_32x32x16_bf16 v[96:111], v[172:175], a[104:107], v[96:111]\n"
    "ds_write_b128 v17, v[72:75]\n"
    "v_mfma_f32_32x32x16_bf16 v[112:127], v[172:175], a[108:111], v[112:127]\n"
    "ds_write_b128 v17, v[88:91] offset:1024\n"
    "s_waitcnt vmcnt(7)\n"
    "v_mfma_f32_32x32x16_bf16 v[96:111], v[176:179], a[112:115], v[96:111]\n"
    "ds_write_b128 v17, v[76:79] offset:2048\n"
    "v_mfma_f32_32x32x16_bf16 v[112:127], v[176:179], a[116:119], v[112:127]\n"
    "ds_write_b128 v17, v[92:95] offset:3072\n"
    "s_waitcnt vmcnt(6)\n"
    "v_mfma_f32_32x32x16_bf16 v[96:111], v[180:183], a[120:123], v[96:111]\n"
    "v_mfma_f32_32x32x16_bf16 v[112:127], v[180:183], a[124:127], v[112:127]\n"
    "s_waitcnt lgkmcnt(0)\n"
    "s_barrier\n"
    "ds_read_b128 v[64:67], v18 offset:4096\n"
    "ds_read_b128 v[68:71], v18 offset:5120\n"
    "ds_read_b128 v[80:83], v18 offset:8192\n"
    "ds_read_b128 v[84:87], v18 offset:9216\n"
    "s_waitcnt vmcnt(5)\n"
    "v_mfma_f32_32x32x16_bf16 v[96:111], v[184:187], a[128:131], v[96:111]\n"
    "ds_read_b128 v[72:75], v18 offset:6144\n"
    "ds_read_b128 v[76:79], v18 offset:7168\n"
    "ds_read_b128 v[88:91], v18 offset:10240\n"
    "ds_read_b128 v[92:95], v18 offset:11264\n"
    "v_mfma_f32_32x32x16_bf16 v[112:127], v[184:187], a[132:135], v[112:127]\n"
    "s_waitcnt vmcnt(4)\n"
    "v_mfma_f32_32x32x16_bf16 v[96:111], v[188:191], a[136:139], v[96:111]\n"
    "v_mfma_f32_32x32x16_bf16 v[112:127], v[188:191], a[140:143], v[112:127]\n"
    "s_waitcnt vmcnt(3)\n"
    "v_mfma_f32_32x32x16_bf16 v[96:111], v[192:195], a[144:147], v[96:111]\n"
    "v_mfma_f32_32x32x16_bf16 v[112:127], v[192:195], a[148:151], v[112:127]\n"
    "s_waitcnt vmcnt(2)\n"
    "v_mfma_f32_32x32x16_bf16 v[96:111], v[196:199], a[152:155], v[96:111]\n"
    "v_mfma_f32_32x32x16_bf16 v[112:127], v[196:199], a[156:159], v[112:127]\n"
    "s_waitcnt vmcnt(1)\n"
    "v_mfma_f32_32x32x16_bf16 v[96:111], v[200:203], a[160:163], v[96:111]\n"
    "v_mfma_f32_32x32x16_bf16 v[112:127], v[200:203], a[164:167], v[112:127]\n"
    "s_waitcnt vmcnt(0)\n"
    "v_mfma_f32_32x32x16_bf16 v[96:111], v[204:207], a[168:171], v[96:111]\n"
    "v_mfma_f32_32x32x16_bf16 v[112:127], v[204:207], a[172:175], v[112:127]\n"
    "s_waitcnt lgkmcnt(5)\n"
    "v_pk_add_f32 v[64:65], v[64:65], v[80:81]\n"
    "v_pk_add_f32 v[66:67], v[66:67], v[82:83]\n"
    "s_waitcnt lgkmcnt(4)\n"
    "v_pk_add_f32 v[68:69], v[68:69], v[84:85]\n"
    "v_pk_add_f32 v[70:71], v[70:71], v[86:87]\n"
    "s_waitcnt lgkmcnt(1)\n"
    "v_pk_add_f32 v[72:73], v[72:73], v[88:89]\n"
    "v_pk_add_f32 v[74:75], v[74:75], v[90:91]\n"
    "s_waitcnt lgkmcnt(0)\n"
    "v_pk_add_f32 v[76:77], v[76:77], v[92:93]\n"
    "v_pk_add_f32 v[78:79], v[78:79], v[94:95]\n"
    "ds_write_b128 v15, v[104:107]\n"
    "ds_write_b128 v15, v[108:111] offset:2048\n"
    "ds_write_b128 v15, v[120:123] offset:1024\n"
    "ds_write_b128 v15, v[124:127] offset:3072\n"
    "v_pk_add_f32 v[96:97], v[96:97], v[64:65]\n"
    "v_pk_add_f32 v[98:99], v[98:99], v[66:67]\n"
    "v_pk_add_f32 v[112:113], v[112:113], v[68:69]\n"
    "v_pk_add_f32 v[114:115], v[114:115], v[70:71]\n"
    "v_pk_add_f32 v[100:101], v[100:101], v[72:73]\n"
    "v_pk_add_f32 v[102:103], v[102:103], v[74:75]\n"
    "v_pk_add_f32 v[116:117], v[116:117], v[76:77]\n"
    "v_pk_add_f32 v[118:119], v[118:119], v[78:79]\n"
    "s_waitcnt lgkmcnt(0)\n"
    "s_barrier\n"
    "ds_read_b128 v[128:131], v18\n"
    "ds_read_b128 v[132:135], v18 offset:1024\n"
    "ds_read_b128 v[136:139], v18 offset:2048\n"
    "ds_read_b128 v[140:143], v18 offset:3072\n"
    "s_waitcnt lgkmcnt(3)\n"
    "v_pk_add_f32 v[96:97], v[96:97], v[128:129]\n"
    "v_pk_add_f32 v[98:99], v[98:99], v[130:131]\n"
    "v_pk_fma_f32 v[32:33], v[48:49], v[96:97], v[32:33]\n"
    "v_pk_fma_f32 v[34:35], v[50:51], v[98:99], v[34:35]\n"
    "s_waitcnt lgkmcnt(2)\n"
    "v_pk_add_f32 v[112:113], v[112:113], v[132:133]\n"
    "v_pk_add_f32 v[114:115], v[114:115], v[134:135]\n"
    "v_pk_fma_f32 v[36:37], v[52:53], v[112:113], v[36:37]\n"
    "v_pk_fma_f32 v[38:39], v[54:55], v[114:115], v[38:39]\n"
    "v_cvt_pk_bf16_f32 v184, v32, v36\n"
    "buffer_store_dword v184, v11, s[52:55], s86 offen sc1\n"
    "v_cvt_pk_bf16_f32 v185, v33, v37\n"
    "buffer_store_dword v185, v11, s[52:55], s86 offen offset:1024 sc1\n"
    "v_cvt_pk_bf16_f32 v186, v34, v38\n"
    "buffer_store_dword v186, v11, s[52:55], s86 offen offset:2048 sc1\n"
    "v_cvt_pk_bf16_f32 v187, v35, v39\n"
    "buffer_store_dword v187, v11, s[52:55], s86 offen offset:3072 sc1\n"
    "s_waitcnt lgkmcnt(1)\n"
    "v_pk_add_f32 v[100:101], v[100:101], v[136:137]\n"
    "v_pk_add_f32 v[102:103], v[102:103], v[138:139]\n"
    "v_pk_fma_f32 v[40:41], v[56:57], v[100:101], v[40:41]\n"
    "v_pk_fma_f32 v[42:43], v[58:59], v[102:103], v[42:43]\n"
    "s_waitcnt lgkmcnt(0)\n"
    "v_pk_add_f32 v[116:117], v[116:117], v[140:141]\n"
    "v_pk_add_f32 v[118:119], v[118:119], v[142:143]\n"
    "v_pk_fma_f32 v[44:45], v[60:61], v[116:117], v[44:45]\n"
    "v_pk_fma_f32 v[46:47], v[62:63], v[118:119], v[46:47]\n"
    "s_add_u32 s86, s86, 0x2000\n"
    "v_cvt_pk_bf16_f32 v188, v40, v44\n"
    "buffer_store_dword v188, v11, s[52:55], s86 offen sc1\n"
    "v_cvt_pk_bf16_f32 v189, v41, v45\n"
    "buffer_store_dword v189, v11, s[52:55], s86 offen offset:1024 sc1\n"
    "v_cvt_pk_bf16_f32 v190, v42, v46\n"
    "buffer_store_dword v190, v11, s[52:55], s86 offen offset:2048 sc1\n"
    "v_cvt_pk_bf16_f32 v191, v43, v47\n"
    "buffer_store_dword v191, v11, s[52:55], s86 offen offset:3072 sc1\n"
    ".Lpf_end:\n"
    : "+v"(tb)
    : "s"(__builtin_amdgcn_kernarg_segment_ptr())
    : "s2", "s3", "s4", "s5", "s6", "s7", "s8", "s9", "s10", "s11", "s12", "s13", "s14", "s15", "s16", "s17", "s18", "s19", "s20", "s21", "s22", "s23", "s24", "s25", "s26", "s27", "s28", "s29", "s30", "s31", "s32", "s33", "s34", "s35", "s36", "s37", "s38", "s39", "s40", "s41", "s42", "s43", "s44", "s45", "s46", "s47", "s48", "s49", "s50", "s51", "s52", "s53", "s54", "s55", "s56", "s57", "s58", "s59", "s60", "s61", "s62", "s63", "s64", "s65", "s66", "s67", "s68", "s69", "s70", "s71", "s72", "s73", "s74", "s75", "s76", "s77", "s78", "s79", "s80", "s81", "s82", "s83", "s84", "s85", "s86", "s87", "s88", "s89", "s90", "s91", "s92", "s93", "s94", "s95", "s96", "s97", "s98", "s99", "v1", "v2", "v3", "v4", "v5", "v6", "v7", "v8", "v9", "v10", "v11", "v12", "v13", "v14", "v15", "v16", "v17", "v18", "v19", "v20", "v21", "v22", "v23", "v24", "v25", "v26", "v27", "v28", "v29", "v30", "v31", "v32", "v33", "v34", "v35", "v36", "v37", "v38", "v39", "v40", "v41", "v42", "v43", "v44", "v45", "v46", "v47", "v48", "v49", "v50", "v51", "v52", "v53", "v54", "v55", "v56", "v57", "v58", "v59", "v60", "v61", "v62", "v63", "v64", "v65", "v66", "v67", "v68", "v69", "v70", "v71", "v72", "v73", "v74", "v75", "v76", "v77", "v78", "v79", "v80", "v81", "v82", "v83", "v84", "v85", "v86", "v87", "v88", "v89", "v90", "v91", "v92", "v93", "v94", "v95", "v96", "v97", "v98", "v99", "v100", "v101", "v102", "v103", "v104", "v105", "v106", "v107", "v108", "v109", "v110", "v111", "v112", "v113", "v114", "v115", "v116", "v117", "v118", "v119", "v120", "v121", "v122", "v123", "v124", "v125", "v126", "v127", "v128", "v129", "v130", "v131", "v132", "v133", "v134", "v135", "v136", "v137", "v138", "v139", "v140", "v141", "v142", "v143", "v144", "v145", "v146", "v147", "v148", "v149", "v150", "v151", "v152", "v153", "v154", "v155", "v156", "v157", "v158", "v159", "v160", "v161", "v162", "v163", "v164", "v165", "v166", "v167", "v168", "v169", "v170", "v171", "v172", "v173", "v174", "v175", "v176", "v177", "v178", "v179", "v180", "v181", "v182", "v183", "v184", "v185", "v186", "v187", "v188", "v189", "v190", "v191", "v192", "v193", "v194", "v195", "v196", "v197", "v198", "v199", "v200", "v201", "v202", "v203", "v204", "v205", "v206", "v207", "v208", "v209", "v210", "v211", "v212", "v213", "v214", "v215", "v216", "v217", "v218", "v219", "v220", "v221", "v222", "v223", "v224", "v225", "v226", "v227", "v228", "v229", "v230", "v231", "v232", "v233", "v234", "v235", "v236", "v237", "v238", "v239", "v240", "v241", "v242", "v243", "v244", "v245", "v246", "v247", "v248", "v249", "v250", "v251", "v252", "v253", "v254", "v255", "a0", "a1", "a2", "a3", "a4", "a5", "a6", "a7", "a8", "a9", "a10", "a11", "a12", "a13", "a14", "a15", "a16", "a17", "a18", "a19", "a20", "a21", "a22", "a23", "a24", "a25", "a26", "a27", "a28", "a29", "a30", "a31", "a32", "a33", "a34", "a35", "a36", "a37", "a38", "a39", "a40", "a41", "a42", "a43", "a44", "a45", "a46", "a47", "a48", "a49", "a50", "a51", "a52", "a53", "a54", "a55", "a56", "a57", "a58", "a59", "a60", "a61", "a62", "a63", "a64", "a65", "a66", "a67", "a68", "a69", "a70", "a71", "a72", "a73", "a74", "a75", "a76", "a77", "a78", "a79", "a80", "a81", "a82", "a83", "a84", "a85", "a86", "a87", "a88", "a89", "a90", "a91", "a92", "a93", "a94", "a95", "a96", "a97", "a98", "a99", "a100", "a101", "a102", "a103", "a104", "a105", "a106", "a107", "a108", "a109", "a110", "a111", "a112", "a113", "a114", "a115", "a116", "a117", "a118", "a119", "a120", "a121", "a122", "a123", "a124", "a125", "a126", "a127", "a128", "a129", "a130", "a131", "a132", "a133", "a134", "a135", "a136", "a137", "a138", "a139", "a140", "a141", "a142", "a143", "a144", "a145", "a146", "a147", "a148", "a149", "a150", "a151", "a152", "a153", "a154", "a155", "a156", "a157", "a158", "a159", "a160", "a161", "a162", "a163", "a164", "a165", "a166", "a167", "a168", "a169", "a170", "a171", "a172", "a173", "a174", "a175", "a176", "a177", "a178", "a179", "a180", "a181", "a182", "a183", "a184", "a185", "a186", "a187", "a188", "a189", "a190", "a191", "a192", "a193", "a194", "a195", "a196", "a197", "a198", "a199", "a200", "a201", "a202", "a203", "a204", "a205", "a206", "a207", "a208", "a209", "a210", "a211", "a212", "a213", "a214", "a215", "a216", "a217", "a218", "a219", "a220", "a221", "a222", "a223", "a224", "a225", "a226", "a227", "a228", "a229", "a230", "a231", "a232", "a233", "a234", "a235", "a236", "a237", "a238", "a239", "a240", "a241", "a242", "a243", "a244", "a245", "a246", "a247", "a248", "a249", "a250", "a251", "a252", "a253", "a254", "a255", "vcc", "scc", "memory");
}

extern "C" void kernel_launch(void* const* d_in, const int* in_sizes, int n_in, void* d_out, int out_size, void* d_ws, size_t ws_size,
                              hipStream_t stream) {
  const unsigned F = 0x00020000u;
  fused_block<<<256, 256, 0, stream>>>(d_in[7], 1024u, F, d_in[8], 3145728u, F, d_in[0], 2097152u, F, d_ws, 0x1300000u, F, d_in[1], 1572864u, F,
                                       d_in[2], 16384u, F, d_in[3], 524288u, F, d_in[5], 1409024u, F, d_in[6], 1409024u, F, d_out, 2097152u, F);
}
```
